```python
import torch

def _softmax(x, axis=-1):
    return torch.softmax(x, dim=axis)

def _gelu(x):
    return torch.nn.functional.gelu(x, approximate='tanh')

def _silu(x):
    return torch.nn.functional.silu(x)

def _sigmoid(x):
    return torch.sigmoid(x)

def _layernorm(x, axis=-1, eps=1e-05):
    m = x.mean(dim=axis, keepdim=True)
    v = x.var(dim=axis, keepdim=True, unbiased=False)
    return (x - m) / torch.sqrt(v + eps)

def _scatter_add(tgt, idx, src, axis=0):
    return tgt.clone().index_add_(axis, idx, src)

def _segment_sum(src, seg, n):
    out = torch.zeros((n,) + src.shape[1:], dtype=src.dtype)
    return out.index_add_(0, seg, src)

def _pad(x, pw):
    flat = tuple((v for pair in reversed(pw) for v in pair))
    return torch.nn.functional.pad(x, flat)
kernel_name = 'gated_mix__memory_aug_recurrent_mem_tr__sliding_block__prompt'

def setup_inputs():

    _w = lambda a: (lambda t: t.to(torch.bfloat16) if t.is_floating_point() else t)(torch.as_tensor(a))

    def _A_setup():
        B, S, Dm = (1, 2048, 512)
        H, Dh = (32, 128)
        M, seg = (8, 32)
        seg = min(seg, S)
        S = S // seg * seg
        return {'x': _w((lambda *s: torch.randn(*s, dtype=torch.bfloat16) * 0.05)(B, S, Dm)), 'mem0': _w((lambda *s: torch.randn(*s, dtype=torch.bfloat16) * 1.6)(B, M, Dm)), 'Wq': _w((lambda *s: torch.randn(*s, dtype=torch.bfloat16) * 0.8)(Dm, H, Dh) * Dm ** (-0.5)), 'Wk': _w((lambda *s: torch.randn(*s, dtype=torch.bfloat16) * 0.8)(Dm, H, Dh) * Dm ** (-0.5)), 'Wv': _w((lambda *s: torch.randn(*s, dtype=torch.bfloat16) * 1.6)(Dm, H, Dh) * Dm ** (-0.5)), 'Wo': _w((lambda *s: torch.randn(*s, dtype=torch.bfloat16) * 1.6)(H, Dh, Dm) * (H * Dh) ** (-0.5)), 'seg_len': seg}
    _A_in = _A_setup()

    def _B_setup():
        B, S, D = (1, 2048, 512)
        return {'x': _w((lambda *s: torch.randn(*s, dtype=torch.bfloat16) * 0.05)(B, S, D)), 'wqkv': _w((lambda *s: torch.randn(*s, dtype=torch.bfloat16) * 1.6)(D, 3 * D)), 'wo': _w((lambda *s: torch.randn(*s, dtype=torch.bfloat16) * 0.05)(D, D))}
    _B_in = _B_setup()
    return {'A_x': _A_in['x'], 'A_mem0': _A_in['mem0'], 'A_Wq': _A_in['Wq'], 'A_Wk': _A_in['Wk'], 'A_Wv': _A_in['Wv'], 'A_Wo': _A_in['Wo'], 'A_seg_len': _A_in['seg_len'], 'B_x': _B_in['x'], 'B_wqkv': _B_in['wqkv'], 'B_wo': _B_in['wo'], 'gate_w': _w((lambda *s: torch.randn(*s, dtype=torch.bfloat16) * 0.05)(512) * 16.0)}

def reference(A_x, A_mem0, A_Wq, A_Wk, A_Wv, A_Wo, A_seg_len, B_x, B_wqkv, B_wo, gate_w):

    def _A_ref(x, mem0, Wq, Wk, Wv, Wo, seg_len):
        B, S, Dm = x.shape
        H, Dh = (Wq.shape[1], Wq.shape[2])
        M = mem0.shape[1]
        scale = Dh ** (-0.5)
        n_seg = S // seg_len
        L = M + seg_len
        idx = torch.arange(L)
        mask = (idx[:, None] < M) | (idx[:, None] >= idx[None, :])
        mem = mem0
        outs = []
        for s in range(n_seg):
            xs = x[:, s * seg_len:(s + 1) * seg_len, :].clone()
            h = torch.concatenate([mem, xs], axis=1)
            q = torch.einsum('btd,dhe->bhte', h, Wq)
            k = torch.einsum('btd,dhe->bhte', h, Wk)
            v = torch.einsum('btd,dhe->bhte', h, Wv)
            scores = torch.einsum('bhqe,bhke->bhqk', q, k) * scale
            scores = torch.where(mask[None, None], scores, -1000000000.0)
            p = _softmax(scores, axis=-1)
            o = torch.einsum('bhqk,bhke->bhqe', p, v)
            o = torch.einsum('bhte,hed->btd', o, Wo)
            mem = o[:, :M, :].clone()
            outs.append(o[:, M:, :])
        return torch.concatenate(outs, axis=1)

    def _B_ref(x, wqkv, wo):
        B, S, D = x.shape
        H = 32
        Dh = D // H
        W = 256
        qkv = (lambda x, *d: x.permute(*d))((x @ wqkv).reshape(B, S, 3, H, Dh), 2, 0, 3, 1, 4)
        q, k, v = (qkv[0], qkv[1], qkv[2])
        scores = torch.einsum('bhqd,bhkd->bhqk', q, k) * Dh ** (-0.5)
        idx = torch.arange(S)
        mask = (idx[:, None] - idx[None, :] >= 0) & (idx[:, None] - idx[None, :] < W)
        scores = torch.where(mask[None, None], scores, -1000000000.0)
        p = _softmax(scores, axis=-1)
        o = torch.einsum('bhqk,bhkd->bhqd', p, v)
        return (lambda x, *d: x.permute(*d))(o, 0, 2, 1, 3).reshape(B, S, D) @ wo
    x = A_x
    g = _sigmoid((x * gate_w).sum(-1, keepdims=True))
    a = _A_ref(x, A_mem0, A_Wq, A_Wk, A_Wv, A_Wo, A_seg_len)
    b = _B_ref(x, B_wqkv, B_wo)
    return g * a + (1 - g) * b
```

```cpp
#include <hip/hip_runtime.h>
#include <stdint.h>
typedef unsigned short u16;
typedef unsigned v4u __attribute__((ext_vector_type(4)));
typedef unsigned v2u __attribute__((ext_vector_type(2)));
typedef float v4f __attribute__((ext_vector_type(4)));
typedef float v2f __attribute__((ext_vector_type(2)));
typedef __bf16 v8bf __attribute__((ext_vector_type(8)));
typedef __bf16 v2bf __attribute__((ext_vector_type(2)));
#define DI __device__ __forceinline__
#define LAS __attribute__((address_space(3)))
#define GAS __attribute__((address_space(1)))
#define BC(T, x) __builtin_bit_cast(T, x)
#define RLX __ATOMIC_RELAXED, __HIP_MEMORY_SCOPE_AGENT
typedef __amdgpu_buffer_rsrc_t rsrc_t;

constexpr unsigned MB = 1u << 20;
constexpr unsigned WS_COMM = 2 * MB, WS_KX = 4 * MB, WS_VX = 20 * MB, WS_KM = 36 * MB, WS_VM = 40 * MB, WS_OA = 44 * MB,
                   WS_QB = 60 * MB, WS_KB = 62 * MB, WS_VB = 64 * MB, WS_OB = 66 * MB, WS_G = 68 * MB, WS_DBG = 69 * MB, WS_XB = 72 * MB, WS_END = 76 * MB;
constexpr unsigned C_SEL = 128, C_ABORT = 256, C_BCNT = 384, C_KVF = 8 * 128, C_KMF = 72 * 128, C_VMF = 104 * 128, C_OC = 136 * 128,
                   C_XF = 256 * 128, CTL_LINES = 288;
constexpr unsigned C_E = 300 * 128, C_Z = 301 * 128, C_SELW = 302 * 128, C_CLAIM = 304 * 128, C_ARR = 320 * 128;
constexpr int LDS_BYTES = 163840;
#define NA 3
#define ND 2
#define ANT 0
#define ROT 1
#define BST 6
constexpr int NB = 64, NB1 = 32;
constexpr unsigned SPIN = 400000;

struct Params {
  const u16 *x, *mem0, *Wq, *Wk, *Wv, *Wo, *wqkv, *wo, *gw;
  u16* out;
  char* ws;
  int P1, P2;
  int da, db, dd, pad;
};

DI unsigned pk(float a, float b) { v2bf r = {(__bf16)a, (__bf16)b}; return BC(unsigned, r); }
DI v4f mf(v4u a, v4u b, v4f c) { return __builtin_amdgcn_mfma_f32_16x16x32_bf16(BC(v8bf, a), BC(v8bf, b), c, 0, 0, 0); }
DI unsigned plo(unsigned y, unsigned x) { return __builtin_amdgcn_perm(y, x, 0x05040100u); }
DI unsigned phi(unsigned y, unsigned x) { return __builtin_amdgcn_perm(y, x, 0x07060302u); }
DI float xmax(float x) {
  auto s = __builtin_amdgcn_permlane16_swap(__float_as_uint(x), __float_as_uint(x), false, false);
  x = fmaxf(__uint_as_float(s[0]), __uint_as_float(s[1]));
  auto t = __builtin_amdgcn_permlane32_swap(__float_as_uint(x), __float_as_uint(x), false, false);
  return fmaxf(__uint_as_float(t[0]), __uint_as_float(t[1]));
}
DI float xsum(float x) {
  auto s = __builtin_amdgcn_permlane16_swap(__float_as_uint(x), __float_as_uint(x), false, false);
  x = __uint_as_float(s[0]) + __uint_as_float(s[1]);
  auto t = __builtin_amdgcn_permlane32_swap(__float_as_uint(x), __float_as_uint(x), false, false);
  return __uint_as_float(t[0]) + __uint_as_float(t[1]);
}
DI v4u ldw(rsrc_t r, unsigned vo, unsigned so) { return __builtin_amdgcn_raw_buffer_load_b128(r, vo, so, 16); }
DI v2u ldw2(rsrc_t r, unsigned vo, unsigned so) { return __builtin_amdgcn_raw_buffer_load_b64(r, vo, so, 16); }
DI void stw(v4u v, rsrc_t r, unsigned vo, unsigned so) { __builtin_amdgcn_raw_buffer_store_b128(v, r, vo, so, 16); }
DI void drain() { asm volatile("s_waitcnt vmcnt(0)" ::: "memory"); }
DI void bar() { asm volatile("s_waitcnt lgkmcnt(0)" ::: "memory"); __builtin_amdgcn_s_barrier(); asm volatile("" ::: "memory"); }
DI int lane_id() { int l; asm volatile("v_mbcnt_lo_u32_b32 %0, -1, 0\n v_mbcnt_hi_u32_b32 %0, -1, %0" : "=v"(l)); return l; }
DI GAS unsigned* ctl(char* ws, unsigned off) { return (GAS unsigned*)(ws + off); }
DI bool wait_ge(char* ws, unsigned off, unsigned want) {
  for (unsigned it = 0; it < SPIN; ++it) {
    unsigned v = __builtin_amdgcn_readfirstlane(__hip_atomic_load(ctl(ws, off), RLX));
    if (v >= want) { asm volatile("" ::: "memory"); return true; }
    if ((it & 63) == 63 && __builtin_amdgcn_readfirstlane(__hip_atomic_load(ctl(ws, C_ABORT), RLX))) return false;
    __builtin_amdgcn_s_sleep(2);
  }
  __hip_atomic_store(ctl(ws, C_ABORT), 1u, RLX);
  return false;
}

#define TSC(slot) do {} while (0)
#define TS(slot) do {} while (0)
#define TSA(s) do {} while (0)
#define TSB(s) do {} while (0)
#define TSD(s) do {} while (0)
typedef unsigned long long u64;
DI GAS u64* ctl64(char* ws, unsigned off) { return (GAS u64*)(ws + off); }
DI u64 uni64(u64 v) { return ((u64)(unsigned)__builtin_amdgcn_readfirstlane((unsigned)(v >> 32)) << 32) | (unsigned)__builtin_amdgcn_readfirstlane((unsigned)v); }
DI u64 mk_tag(u64 ev) { return (uni64(ev) + 1) & 0x7fffffffffffffffull; }
DI bool wait_z(char* ws, u64 tag) {
  for (unsigned it = 0; it < SPIN; ++it) {
    if (uni64(__hip_atomic_load(ctl64(ws, C_Z), RLX)) == tag) { asm volatile("" ::: "memory"); return true; }
    __builtin_amdgcn_s_sleep(1);
  }
  return false;
}
DI bool wait_arrivals(char* ws, u64 tag, int lane, u64 (&orphan)[4]) {
  for (unsigned it = 0; it < SPIN; ++it) {
    bool ok = true;
#pragma unroll
    for (int j = 0; j < 4; ++j) {
      const u64 v = __hip_atomic_load(ctl64(ws, C_ARR + (64 * j + lane) * 8), RLX);
      ok = ok && (v >> 1) == tag;
      orphan[j] = __ballot(v & 1);
    }
    if (__all(ok)) { asm volatile("" ::: "memory"); return true; }
    __builtin_amdgcn_s_sleep(2);
  }
  return false;
}
DI u64 claim_try(char* ws, int slot, u64 tag, int lane) {
  u64 old = 0;
  if (lane == 0) old = __hip_atomic_exchange(ctl64(ws, C_CLAIM + slot * 8), tag, RLX);
  return old;
}
DI bool claim_lost(char* ws, u64 old, u64 tag, int lane) {
  const bool lost = uni64(old) == tag;
  if (lane == 0) __hip_atomic_store(ctl64(ws, C_ARR + blockIdx.x * 8), (tag << 1) | (lost ? 1u : 0u), RLX);
  return lost;
}
DI bool share(LAS char* word, bool v, int w) {
  if (w == 0) *(LAS unsigned*)word = v;
  bar();
  const bool r = __builtin_amdgcn_readfirstlane(*(LAS unsigned*)word);
  bar();
  return r;
}
DI int adopt_role(char* ws, u64 tag, int lane) {
  const int b = blockIdx.x;
  u64 orphan[4];
  if (!wait_arrivals(ws, tag, lane, orphan)) return -1;
  int rank = 0;
#pragma unroll
  for (int j = 0; j < 4; ++j) {
    const int hi = b - 64 * j;
    rank += hi <= 0 ? 0 : __popcll(hi >= 64 ? orphan[j] : orphan[j] & ((1ull << hi) - 1));
  }
  for (int j = 0; j < 4; ++j) {
    u64 fr = __ballot(__hip_atomic_load(ctl64(ws, C_CLAIM + (64 * j + lane) * 8), RLX) != tag);
    const int c = __popcll(fr);
    if (rank < c) { for (; rank > 0; --rank) fr &= fr - 1; return 64 * j + __ffsll((long long)fr) - 1; }
    rank -= c;
  }
  return -1;
}
DI void init_ctl(char* ws, u64 tag, int lane) {
#pragma unroll
  for (int j = 0; j < 5; ++j)
    if (64 * j + lane < (int)CTL_LINES) __hip_atomic_store(ctl64(ws, (64 * j + lane) * 128), 0ull, RLX);
  drain();
  if (lane == 0) __hip_atomic_store(ctl64(ws, C_Z), tag, RLX);
}
DI void close_ctl(char* ws, u64 tag, int lane) {
  u64 orphan[4];
  if (wait_arrivals(ws, tag, lane, orphan) && lane == 0) __hip_atomic_store(ctl64(ws, C_E), tag, RLX);
}
template <int AUX = 0, int I0 = 0, int I1 = 32>
DI void load_w(const u16* W, int rs, int w, int lane, v4u (&R)[32], int rot = 0) {
  const rsrc_t r = __builtin_amdgcn_make_buffer_rsrc((void*)W, 0, 512 * rs * 2, 0x00020000);
  const unsigned vo = (unsigned)(8 * (lane >> 4) * rs + 8 * (lane & 15)) * 2u;
  unsigned so = 0;
#pragma unroll
  for (int i = I0; i < I1; ++i) {
    if ((i & 7) == 0) so = (unsigned)((128 * w + 32 * (((i >> 3) + rot) & 3)) * rs) * 2u;
    R[i] = __builtin_amdgcn_raw_buffer_load_b128(r, vo, so, AUX);
    so += rs * 2;
    asm volatile("" : "+s"(so));
  }
  __builtin_amdgcn_sched_barrier(0);
}
template <int MAP, int G0 = 0, int G1 = 4>
DI void stage_put(const v4u (&R)[32], LAS char* lds, int w, int lane, int rot = 0) {
  const int n0 = lane & 15, g = lane >> 4;
#pragma unroll
  for (int kq = G0; kq < G1; ++kq) {
#pragma unroll
    for (int x = 0; x < 8; ++x) {
      v4u f;
#pragma unroll
      for (int q = 0; q < 4; ++q) {
        unsigned a = R[8 * kq + 2 * q][x >> 1], b = R[8 * kq + 2 * q + 1][x >> 1];
        f[q] = (x & 1) ? phi(b, a) : plo(b, a);
      }
      int tile, slot;
      if (MAP == 0) { tile = x; slot = n0; }
      else if (MAP == 1) { tile = 2 * (n0 >> 2) + (x & 1); slot = 4 * (n0 & 3) + (x >> 1); }
      else { tile = n0 >> 1; slot = 8 * (n0 & 1) + x; }
      *(LAS v4u*)(lds + (tile * 16 + 4 * w + ((kq + rot) & 3)) * 1024 + (g * 16 + slot) * 16) = f;
    }
    __builtin_amdgcn_sched_barrier(0);
  }
}
DI void stage_get(LAS char* lds, int w, int lane, v4u (&F0)[16], v4u (&F1)[16]) {
  bar();
#pragma unroll
  for (int k = 0; k < 16; ++k) {
    F0[k] = *(LAS v4u*)(lds + ((2 * w) * 16 + k) * 1024 + lane * 16);
    F1[k] = *(LAS v4u*)(lds + ((2 * w + 1) * 16 + k) * 1024 + lane * 16);
  }
  bar();
}
constexpr int NVA = 6;
DI void pin_kv(v4u (&K0)[16], v4u (&K1)[16], v4u (&V0)[16], v4u (&V1)[16]) {
#pragma unroll
  for (int k = 0; k < 16; ++k) {
    asm volatile("" : "+a"(K0[k]), "+a"(K1[k]), "+a"(V0[k]));
    if (k < NVA) asm volatile("" : "+a"(V1[k])); else asm volatile("" : "+v"(V1[k]));
  }
}
DI void dma_x(rsrc_t rx, int s, unsigned dst, int w, unsigned vo) {
  const unsigned so = (unsigned)(32 * s + 16 * (w >> 1)) * 1024u + (unsigned)(w & 1) * 512u, m = dst + (unsigned)w * 8192u;
  unsigned keep;
  asm volatile(
      "s_mov_b32 %0, m0\n s_mov_b32 m0, %4\n s_nop 0\n"
      "buffer_load_dwordx4 %1, %2, %3 offen lds\n s_add_u32 m0, m0, 960\n s_nop 0\n"
      "buffer_load_dwordx4 %1, %2, %3 offen offset:64 lds\n s_add_u32 m0, m0, 960\n s_nop 0\n"
      "buffer_load_dwordx4 %1, %2, %3 offen offset:128 lds\n s_add_u32 m0, m0, 960\n s_nop 0\n"
      "buffer_load_dwordx4 %1, %2, %3 offen offset:192 lds\n s_add_u32 m0, m0, 960\n s_nop 0\n"
      "buffer_load_dwordx4 %1, %2, %3 offen offset:256 lds\n s_add_u32 m0, m0, 960\n s_nop 0\n"
      "buffer_load_dwordx4 %1, %2, %3 offen offset:320 lds\n s_add_u32 m0, m0, 960\n s_nop 0\n"
      "buffer_load_dwordx4 %1, %2, %3 offen offset:384 lds\n s_add_u32 m0, m0, 960\n s_nop 0\n"
      "buffer_load_dwordx4 %1, %2, %3 offen offset:448 lds\n s_mov_b32 m0, %0"
      : "=&s"(keep) : "v"(vo), "s"(rx), "s"(so), "s"(m) : "memory", "scc");
}
#define MF_WA(acc, wt, xx) asm volatile("s_nop 1\n v_mfma_f32_16x16x32_bf16 %0, %1, %2, %0" : "+v"(acc) : "a"(wt), "v"(xx))
#define MF_WB(acc, xx, wt) asm volatile("s_nop 1\n v_mfma_f32_16x16x32_bf16 %0, %1, %2, %0" : "+v"(acc) : "v"(xx), "a"(wt))
#define MF_WV(acc, xx, wt) asm volatile("s_nop 1\n v_mfma_f32_16x16x32_bf16 %0, %1, %2, %0" : "+v"(acc) : "v"(xx), "v"(wt))
DI void kv_gemm(LAS char* xl, int lane, const v4u (&K0)[16], const v4u (&K1)[16], const v4u (&V0)[16], const v4u (&V1)[16], v4u (&rk)[2],
                v4u (&rv)[2]) {
  v4f a[8];
#pragma unroll
  for (int i = 0; i < 8; ++i) a[i] = (v4f){0, 0, 0, 0};
  v4u x0 = *(LAS v4u*)(xl + lane * 16), x1 = *(LAS v4u*)(xl + 16384 + lane * 16);
  asm volatile("s_nop 1" : "+v"(a[0]), "+v"(a[1]), "+v"(a[2]), "+v"(a[3]), "+v"(a[4]), "+v"(a[5]), "+v"(a[6]), "+v"(a[7]));
#pragma unroll
  for (int k = 0; k < 16; ++k) {
    v4u y0 = x0, y1 = x1;
    if (k < 15) { y0 = *(LAS v4u*)(xl + (k + 1) * 1024 + lane * 16); y1 = *(LAS v4u*)(xl + (17 + k) * 1024 + lane * 16); }
    __builtin_amdgcn_sched_barrier(0);
    MF_WA(a[0], K0[k], x0); MF_WA(a[1], K1[k], x0); MF_WB(a[4], x0, V0[k]);
    if (k < NVA) MF_WB(a[6], x0, V1[k]); else MF_WV(a[6], x0, V1[k]);
    MF_WA(a[2], K0[k], x1); MF_WA(a[3], K1[k], x1); MF_WB(a[5], x1, V0[k]);
    if (k < NVA) MF_WB(a[7], x1, V1[k]); else MF_WV(a[7], x1, V1[k]);
    __builtin_amdgcn_sched_barrier(0);
    x0 = y0; x1 = y1;
  }
  asm volatile("s_nop 7" : "+v"(a[0]), "+v"(a[1]), "+v"(a[2]), "+v"(a[3]), "+v"(a[4]), "+v"(a[5]), "+v"(a[6]), "+v"(a[7]));
#pragma unroll
  for (int t = 0; t < 2; ++t) {
#pragma unroll
    for (int i = 0; i < 4; ++i) rk[t][i] = pk(a[2 * t][i], a[2 * t + 1][i]);
    rv[t] = (v4u){pk(a[4 + 2 * t][0], a[4 + 2 * t][1]), pk(a[4 + 2 * t][2], a[4 + 2 * t][3]), pk(a[5 + 2 * t][0], a[5 + 2 * t][1]), pk(a[5 + 2 * t][2], a[5 + 2 * t][3])};
  }
}
#define MF0_WA(acc, wt, xx) asm volatile("s_nop 1\n v_mfma_f32_16x16x32_bf16 %0, %1, %2, 0" : "=&v"(acc) : "a"(wt), "v"(xx))
#define MF0_WB(acc, xx, wt) asm volatile("s_nop 1\n v_mfma_f32_16x16x32_bf16 %0, %1, %2, 0" : "=&v"(acc) : "v"(xx), "a"(wt))
#define MF0_WV(acc, xx, wt) asm volatile("s_nop 1\n v_mfma_f32_16x16x32_bf16 %0, %1, %2, 0" : "=&v"(acc) : "v"(xx), "v"(wt))
DI void qkv_gemm(LAS char* xl, int lane, const v4u (&Q0)[16], const v4u (&Q1)[16], const v4u (&K0)[16], const v4u (&K1)[16],
                 const v4u (&V0)[16], const v4u (&V1)[16], v4u (&rq)[2], v4u (&rk)[2], v4u (&rv)[2]) {
  v4f a[12];
  v4u x0 = *(LAS v4u*)(xl + lane * 16), x1 = *(LAS v4u*)(xl + 16384 + lane * 16);
#pragma unroll
  for (int k = 0; k < 16; ++k) {
    v4u y0 = x0, y1 = x1;
    if (k < 15) { y0 = *(LAS v4u*)(xl + (k + 1) * 1024 + lane * 16); y1 = *(LAS v4u*)(xl + (17 + k) * 1024 + lane * 16); }
    __builtin_amdgcn_sched_barrier(0);
    if (k == 0) {
      MF0_WA(a[0], K0[k], x0); MF0_WA(a[1], K1[k], x0); MF0_WB(a[4], x0, V0[k]); MF0_WB(a[6], x0, V1[k]);
      MF0_WV(a[8], Q0[k], x0); MF0_WV(a[9], Q1[k], x0);
      MF0_WA(a[2], K0[k], x1); MF0_WA(a[3], K1[k], x1); MF0_WB(a[5], x1, V0[k]); MF0_WB(a[7], x1, V1[k]);
      MF0_WV(a[10], Q0[k], x1); MF0_WV(a[11], Q1[k], x1);
    } else {
      MF_WV(a[8], Q0[k], x0); MF_WV(a[9], Q1[k], x0);
      MF_WA(a[0], K0[k], x0); MF_WA(a[1], K1[k], x0); MF_WB(a[4], x0, V0[k]);
      if (k < NVA) MF_WB(a[6], x0, V1[k]); else MF_WV(a[6], x0, V1[k]);
      MF_WV(a[10], Q0[k], x1); MF_WV(a[11], Q1[k], x1);
      MF_WA(a[2], K0[k], x1); MF_WA(a[3], K1[k], x1); MF_WB(a[5], x1, V0[k]);
      if (k < NVA) MF_WB(a[7], x1, V1[k]); else MF_WV(a[7], x1, V1[k]);
    }
    __builtin_amdgcn_sched_barrier(0);
    x0 = y0; x1 = y1;
  }
  asm volatile("s_nop 7" : "+v"(a[0]), "+v"(a[1]), "+v"(a[2]), "+v"(a[3]), "+v"(a[4]), "+v"(a[5]), "+v"(a[6]), "+v"(a[7]), "+v"(a[8]),
               "+v"(a[9]), "+v"(a[10]), "+v"(a[11]));
#pragma unroll
  for (int t = 0; t < 2; ++t) {
#pragma unroll
    for (int i = 0; i < 4; ++i) { rk[t][i] = pk(a[2 * t][i], a[2 * t + 1][i]); rq[t][i] = pk(a[8 + 2 * t][i], a[9 + 2 * t][i]); }
    rv[t] = (v4u){pk(a[4 + 2 * t][0], a[4 + 2 * t][1]), pk(a[4 + 2 * t][2], a[4 + 2 * t][3]), pk(a[5 + 2 * t][0], a[5 + 2 * t][1]), pk(a[5 + 2 * t][2], a[5 + 2 * t][3])};
  }
}
DI void q_gemm(LAS char* xl, int lane, const v4u (&Q0)[16], const v4u (&Q1)[16], v4u (&rq)[2]) {
  v4f a[4];
#pragma unroll
  for (int i = 0; i < 4; ++i) a[i] = (v4f){0, 0, 0, 0};
  v4u X0[3], X1[3];
#pragma unroll
  for (int i = 0; i < 2; ++i) { X0[i] = *(LAS v4u*)(xl + i * 1024 + lane * 16); X1[i] = *(LAS v4u*)(xl + (16 + i) * 1024 + lane * 16); }
#pragma unroll
  for (int k = 0; k < 16; ++k) {
    if (k < 14) { X0[(k + 2) % 3] = *(LAS v4u*)(xl + (k + 2) * 1024 + lane * 16); X1[(k + 2) % 3] = *(LAS v4u*)(xl + (18 + k) * 1024 + lane * 16); }
    __builtin_amdgcn_sched_barrier(0);
    a[0] = mf(Q0[k], X0[k % 3], a[0]); a[1] = mf(Q1[k], X0[k % 3], a[1]); a[2] = mf(Q0[k], X1[k % 3], a[2]); a[3] = mf(Q1[k], X1[k % 3], a[3]);
    __builtin_amdgcn_sched_barrier(0);
  }
#pragma unroll
  for (int t = 0; t < 2; ++t)
#pragma unroll
    for (int i = 0; i < 4; ++i) rq[t][i] = pk(a[2 * t][i], a[2 * t + 1][i]);
}

DI void put_kv(rsrc_t rw, const v4u (&rk)[2], const v4u (&rv)[2], int sh, int w, int lane) {
  const unsigned so = (unsigned)sh * 8192u;
#pragma unroll
  for (int t = 0; t < 2; ++t) {
    stw(rk[t], rw, (t * 4 + w) * 1024 + lane * 16, WS_KX + so);
    stw(rv[t], rw, (2 * w + t) * 1024 + lane * 16, WS_VX + so);
  }
  __builtin_amdgcn_raw_buffer_store_b64((v2u){~0u, ~0u}, rw, w * 512 + lane * 8, WS_KM + so / 4, 16);
  __builtin_amdgcn_raw_buffer_store_b64((v2u){~0u, ~0u}, rw, w * 512 + lane * 8, WS_VM + so / 4, 16);
}
DI bool role_a(const Params& p, char* ws, LAS char* lds, int h, int par, int w, int slot, u64 ev, u64& tag) {
  const rsrc_t rw = __builtin_amdgcn_make_buffer_rsrc(ws, 0, WS_END, 0x00020000);
  const rsrc_t rx = __builtin_amdgcn_make_buffer_rsrc((void*)p.x, 0, 2048 * 1024, 0x00020000);
  v4u Q0[16], Q1[16], K0[16], K1[16], V0[16], V1[16];
  v4u rk[2], rv[2], rq[2], rk1[2], rv1[2];
  {
    v4u R1[32], R2[32];
    { const int l = lane_id(); dma_x(rx, par, 131072, w, (l & 15) * 1024 + (l >> 4) * 16); }
    load_w<ANT>(p.Wk + h * 128, 4096, w, lane_id(), R1, ROT * par);
    tag = mk_tag(ev);
    u64 old = 0;
    if (w == 0 && slot >= 0) old = claim_try(ws, slot, tag, lane_id());
    __builtin_amdgcn_sched_barrier(0);
    load_w<ANT, 0, 16>(p.Wv + h * 128, 4096, w, lane_id(), R2, ROT * par);
    TSA(112);
    stage_put<0, 0, 1>(R1, lds, w, lane_id(), ROT * par);
    load_w<ANT, 16, 24>(p.Wv + h * 128, 4096, w, lane_id(), R2, ROT * par);
    stage_put<0, 1, 2>(R1, lds, w, lane_id(), ROT * par);
    load_w<ANT, 24, 32>(p.Wv + h * 128, 4096, w, lane_id(), R2, ROT * par);
    stage_put<0, 2, 4>(R1, lds, w, lane_id(), ROT * par);
    TSA(113);
    stage_get(lds, w, lane_id(), K0, K1);
    if (slot >= 0 && share(lds, w == 0 && claim_lost(ws, old, tag, lane_id()), w)) { drain(); return true; }
    TSA(114);
    stage_put<0>(R2, lds, w, lane_id(), ROT * par);
    TSA(115);
    stage_get(lds, w, lane_id(), V0, V1);
    pin_kv(K0, K1, V0, V1);
    TSA(1);
    const u64 z0 = __hip_atomic_load(ctl64(ws, C_Z), RLX);
    __builtin_amdgcn_sched_barrier(0);
    { const int l = lane_id(); dma_x(rx, NA + par, 0, w, (l & 15) * 1024 + (l >> 4) * 16); }
    kv_gemm(lds + 131072, lane_id(), K0, K1, V0, V1, rk, rv);
    if (uni64(z0) != tag && !wait_z(ws, tag)) return false;
    put_kv(rw, rk, rv, par * 32 + h, w, lane_id());
    __builtin_amdgcn_sched_barrier(0);
    load_w<ANT, 0, 8>(p.Wq + h * 128, 4096, w, lane_id(), R1, ROT * par);
    asm volatile("s_waitcnt vmcnt(8)" ::: "memory");
    bar();
    if (w == 0 && lane_id() == 0) __hip_atomic_fetch_or(ctl(ws, C_KVF + h * 128), 1u << par, RLX);
    TSA(38);
    load_w<ANT, 8, 32>(p.Wq + h * 128, 4096, w, lane_id(), R1, ROT * par);
    kv_gemm(lds, lane_id(), K0, K1, V0, V1, rk1, rv1);
    put_kv(rw, rk1, rv1, (NA + par) * 32 + h, w, lane_id());
    drain(); bar();
    if (w == 0 && lane_id() == 0) __hip_atomic_fetch_or(ctl(ws, C_KVF + h * 128), 1u << (NA + par), RLX);
    TSA(39);
    stage_put<0>(R1, lds, w, lane_id(), ROT * par);
    stage_get(lds, w, lane_id(), Q0, Q1);
  }
  const int lane = lane_id();
  LAS char* ring = lds + 32768;
  const int n = lane & 15, g = lane >> 4, qt = w & 1, eh = w >> 1;
  const float cs = 0.08838834764831845f * 1.4426950408889634f;
  const unsigned xvo = n * 1024 + g * 16;
  const int nit = (63 - par) / NA + 1;
  const unsigned kmo = (2 * (n >> 2) + (n & 1)) * 256 + g * 16, vmo = 2 * eh * 512 + lane * 8;
  dma_x(rx, NA + par, 0, w, xvo);
#pragma unroll
  for (int t = 0; t < 2; ++t) {
    *(LAS v4u*)(ring + lane * 16 + 8192 + (t * 4 + w) * 1024) = rk[t];
    *(LAS v4u*)(ring + lane * 16 + 16384 + (2 * w + t) * 1024) = rv[t];
    *(LAS v4u*)(ring + 24576 + lane * 16 + 8192 + (t * 4 + w) * 1024) = rk1[t];
    *(LAS v4u*)(ring + 24576 + lane * 16 + 16384 + (2 * w + t) * 1024) = rv1[t];
  }
  q_gemm(lds + 131072, lane, Q0, Q1, rq);
#pragma unroll
  for (int t = 0; t < 2; ++t) *(LAS v4u*)(ring + lane * 16 + (t * 4 + w) * 1024) = rq[t];
  TSA(2);
  drain(); bar();
  dma_x(rx, 2 * NA + par, 131072, w, xvo);
  q_gemm(lds, lane, Q0, Q1, rq);
#pragma unroll
  for (int t = 0; t < 2; ++t) *(LAS v4u*)(ring + 24576 + lane * 16 + (t * 4 + w) * 1024) = rq[t];
  TSA(3);
  drain(); bar();
  for (int it = 2; it < nit + 3; ++it) {
    pin_kv(K0, K1, V0, V1);
    const int jt = it - 3, s = NA * jt + par;
    const unsigned mo = (unsigned)(s * 32 + h) * 2048u;
    v4u km[4]; v2u vm[2];
    if (it + 1 < nit) dma_x(rx, NA * (it + 1) + par, (it & 1) ? 131072 : 0, w, xvo);
    if (it >= 3) {
      asm volatile(
          "buffer_load_dwordx4 %0, %6, %8, %9 offen sc1\n buffer_load_dwordx4 %1, %6, %8, %9 offen offset:64 sc1\n"
          "buffer_load_dwordx4 %2, %6, %8, %9 offen offset:128 sc1\n buffer_load_dwordx4 %3, %6, %8, %9 offen offset:192 sc1\n"
          "buffer_load_dwordx2 %4, %7, %8, %10 offen sc1\n buffer_load_dwordx2 %5, %7, %8, %10 offen offset:512 sc1"
          : "=&a"(km[0]), "=&a"(km[1]), "=&a"(km[2]), "=&a"(km[3]), "=&a"(vm[0]), "=&a"(vm[1])
          : "v"(kmo), "v"(vmo), "s"(rw), "s"(WS_KM + mo), "s"(WS_VM + mo) : "memory");
    }
    if (it < nit) {
      LAS char* xl = lds + ((it & 1) ? 0 : 131072);
      if (it == 12) { TSA(120); TSC(124); }
      qkv_gemm(xl, lane, Q0, Q1, K0, K1, V0, V1, rq, rk, rv);
      if (it == 12) { TSA(121); TSC(125); }
    }
    if (it >= 3) {
      asm volatile("s_waitcnt vmcnt(0)" : "+a"(km[0]), "+a"(km[1]), "+a"(km[2]), "+a"(km[3]), "+a"(vm[0]), "+a"(vm[1])::"memory");
    }
    if (it < nit) {
      LAS char* sl = ring + (it & 3) * 24576 + lane * 16;
      put_kv(rw, rk, rv, (NA * it + par) * 32 + h, w, lane);
#pragma unroll
      for (int t = 0; t < 2; ++t) {
        *(LAS v4u*)(sl + 8192 + (t * 4 + w) * 1024) = rk[t];
        *(LAS v4u*)(sl + 16384 + (2 * w + t) * 1024) = rv[t];
        *(LAS v4u*)(sl + (t * 4 + w) * 1024) = rq[t];
      }
      TSA(2 + it);
    }
    if (it >= 3) {
      LAS char* sl = ring + (jt & 3) * 24576 + lane * 16;
      v4f T0 = {0, 0, 0, 0}, T1 = T0, Tm = T0;
      v4u q[4];
#pragma unroll
      for (int kk = 0; kk < 4; ++kk) {
        q[kk] = *(LAS v4u*)(sl + (qt * 4 + kk) * 1024);
        T0 = mf(*(LAS v4u*)(sl + 8192 + kk * 1024), q[kk], T0);
        T1 = mf(*(LAS v4u*)(sl + 8192 + (4 + kk) * 1024), q[kk], T1);
      }
#pragma unroll
      for (int r = 0; r < 4; ++r) {
        const bool up = 4 * g + r > n;
        if (qt == 0) { T0[r] = up ? -INFINITY : T0[r]; T1[r] = -INFINITY; }
        else T1[r] = up ? -INFINITY : T1[r];
      }
      if (it == 12) TSA(116);
      for (unsigned sp = 0;; ++sp) {
        unsigned mx = max(max(vm[0].x, vm[0].y), max(vm[1].x, vm[1].y));
#pragma unroll
        for (int kk = 0; kk < 4; ++kk) mx = max(mx, max(max(km[kk].x, km[kk].y), max(km[kk].z, km[kk].w)));
        if (__all(mx != ~0u)) break;
        for (; s < 63; ++sp) {
          if (sp > SPIN) { __hip_atomic_store(ctl(ws, C_ABORT), 1u, RLX); return false; }
          asm volatile("" ::: "memory");
          if (__all(__builtin_amdgcn_raw_buffer_load_b32(rw, (lane & 3) * 512 + 508, WS_KM + mo, 16) != ~0u)) break;
          __builtin_amdgcn_s_sleep(6);
        }
        if (sp > SPIN) { __hip_atomic_store(ctl(ws, C_ABORT), 1u, RLX); return false; }
        asm volatile("" ::: "memory");
#pragma unroll
        for (int kk = 0; kk < 4; ++kk) km[kk] = ldw(rw, kmo + kk * 64, WS_KM + mo);
#pragma unroll
        for (int u = 0; u < 2; ++u) vm[u] = ldw2(rw, vmo + u * 512, WS_VM + mo);
      }
      TSA(40 + jt);
#pragma unroll
      for (int kk = 0; kk < 4; ++kk) Tm = mf(km[kk], q[kk], Tm);
      float m = fmaxf(fmaxf(fmaxf(T0[0], T0[1]), fmaxf(T0[2], T0[3])), fmaxf(fmaxf(T1[0], T1[1]), fmaxf(T1[2], T1[3])));
      m = xmax(fmaxf(m, fmaxf(Tm[0], Tm[1])));
      const float mc = m * cs;
      float e0[4], e1[4];
#pragma unroll
      for (int r = 0; r < 4; ++r) { e0[r] = __builtin_amdgcn_exp2f(fmaf(T0[r], cs, -mc)); e1[r] = __builtin_amdgcn_exp2f(fmaf(T1[r], cs, -mc)); }
      const float f0 = __builtin_amdgcn_exp2f(fmaf(Tm[0], cs, -mc)), f1 = __builtin_amdgcn_exp2f(fmaf(Tm[1], cs, -mc));
      const v4u Pb = {pk(e0[0], e0[1]), pk(e0[2], e0[3]), pk(e1[0], e1[1]), pk(e1[2], e1[3])}, Pm = {pk(f0, f1), 0, 0, 0};
      const float rl = __builtin_amdgcn_rcpf(xsum(((e0[0] + e0[1]) + (e0[2] + e0[3])) + ((e1[0] + e1[1]) + (e1[2] + e1[3])) + (f0 + f1)));
      v4u of[2];
#pragma unroll
      for (int u = 0; u < 2; ++u) {
        v4f o0 = mf(*(LAS v4u*)(sl + 16384 + (4 * eh + 2 * u) * 1024), Pb, (v4f){0, 0, 0, 0});
        v4f o1 = mf(*(LAS v4u*)(sl + 16384 + (4 * eh + 2 * u + 1) * 1024), Pb, (v4f){0, 0, 0, 0});
        o0 = mf((v4u){vm[u].x, 0, 0, 0}, Pm, o0);
        o1 = mf((v4u){vm[u].y, 0, 0, 0}, Pm, o1);
        of[u] = (v4u){pk(o0[0] * rl, o0[1] * rl), pk(o0[2] * rl, o0[3] * rl), pk(o1[0] * rl, o1[1] * rl), pk(o1[2] * rl, o1[3] * rl)};
      }
      if (it == 12) TSA(118);
      TSA(80 + jt);
#pragma unroll
      for (int u = 0; u < 2; ++u) stw(of[u], rw, lane * 16, WS_OA + (unsigned)(((s * 2 + qt) * 32 + h) * 4 + 2 * eh + u) * 1024u);
    }
    drain(); bar();
    if (w == 0 && lane == 0) {
      const int sk = NA * it + par;
      if (it >= 3) __hip_atomic_fetch_add(ctl(ws, C_OC + s * 128), 1u, RLX);
      if (it < nit) __hip_atomic_fetch_or(ctl(ws, C_KVF + h * 128 + (sk >> 5) * 4), 1u << (sk & 31), RLX);
    }
  }
  return false;
}

DI void role_b1(const Params& p, char* ws, LAS char* lds, int id, int w) {
  const rsrc_t rw = __builtin_amdgcn_make_buffer_rsrc(ws, 0, WS_END, 0x00020000);
  {
    const int grp = id & 3;
    v4u Q0[16], Q1[16], K0[16], K1[16], V0[16], V1[16];
    {
      v4u R1[32], R2[32];
      load_w(p.wqkv + 512 + 128 * grp, 1536, w, lane_id(), R1);
      load_w(p.wqkv + 1024 + 128 * grp, 1536, w, lane_id(), R2);
      stage_put<1>(R1, lds, w, lane_id());
      load_w(p.wqkv + 128 * grp, 1536, w, lane_id(), R1);
      stage_get(lds, w, lane_id(), K0, K1);
      stage_put<2>(R2, lds, w, lane_id());
      stage_get(lds, w, lane_id(), V0, V1);
      stage_put<1>(R1, lds, w, lane_id());
      stage_get(lds, w, lane_id(), Q0, Q1);
      pin_kv(K0, K1, V0, V1);
    }
    const int lane = lane_id(), n = lane & 15, g = lane >> 4;
    int buf = 0;
    TSB(99);
    const rsrc_t rx = __builtin_amdgcn_make_buffer_rsrc((void*)p.x, 0, 2048 * 1024, 0x00020000);
    const unsigned xvo = n * 1024 + g * 16;
    dma_x(rx, id >> 2, 0, w, xvo);
    for (int s = id >> 2; s < 64; s += NB1 / 4) {
      drain(); bar();
      if (s + NB1 / 4 < 64) dma_x(rx, s + NB1 / 4, buf ^ 32768, w, xvo);
      v4u rk[2], rv[2], rq[2];
      qkv_gemm(lds + buf, lane, Q0, Q1, K0, K1, V0, V1, rq, rk, rv);
      const int pr = 4 * grp + w;
#pragma unroll
      for (int t = 0; t < 2; ++t) {
        stw(rq[t], rw, lane * 16, WS_QB + (unsigned)((s * 16 + pr) * 2 + t) * 1024u);
        stw(rk[t], rw, lane * 16, WS_KB + (unsigned)((s * 16 + pr) * 2 + t) * 1024u);
        stw(rv[t], rw, lane * 16, WS_VB + (unsigned)((s * 16 + pr) * 2 + t) * 1024u);
      }
      buf ^= 32768;
    }
    drain(); bar();
    if (w == 0 && lane == 0) __hip_atomic_fetch_add(ctl(ws, C_BCNT), 1u, RLX);
  }
  TSB(100);
}
DI void role_b2(const Params& p, char* ws, int id, int w) {
  const rsrc_t rw = __builtin_amdgcn_make_buffer_rsrc(ws, 0, WS_END, 0x00020000);
  const int lane = lane_id(), n = lane & 15, g = lane >> 4;
  {
    const v4u gwv = *(const v4u*)(p.gw + lane * 8);
    v4u xr[8];
#pragma unroll
    for (int i = 0; i < 8; ++i) xr[i] = *(const v4u*)(p.x + (size_t)(32 * id + 8 * w + i) * 512 + lane * 8);
    float gv = 0.f;
#pragma unroll
    for (int i = 0; i < 8; ++i) {
      float d = 0.f;
#pragma unroll
      for (int j = 0; j < 4; ++j) {
        d = fmaf(__uint_as_float(xr[i][j] << 16), __uint_as_float(gwv[j] << 16), d);
        d = fmaf(__uint_as_float(xr[i][j] & 0xffff0000u), __uint_as_float(gwv[j] & 0xffff0000u), d);
      }
      d += BC(float, __builtin_amdgcn_update_dpp(0, BC(int, d), 0x128, 0xf, 0xf, false));
      d += BC(float, __builtin_amdgcn_update_dpp(0, BC(int, d), 0x124, 0xf, 0xf, false));
      d += BC(float, __builtin_amdgcn_update_dpp(0, BC(int, d), 0x122, 0xf, 0xf, false));
      d += BC(float, __builtin_amdgcn_update_dpp(0, BC(int, d), 0x121, 0xf, 0xf, false));
      d = xsum(d);
      gv = (lane & 7) == i ? d : gv;
    }
    gv = 1.f / (1.f + __expf(-gv));
    if (lane < 32) __builtin_amdgcn_raw_buffer_store_b32(__float_as_uint(gv), rw, lane * 4, WS_G + (unsigned)(id * 4 + w) * 128u, 16);
    drain();
    if (lane == 0) __hip_atomic_fetch_add(ctl(ws, C_OC + id * 128), 1u, RLX);
  }
  TSB(2);
  if (!wait_ge(ws, C_BCNT, NB1)) return;
  TSB(3);
  for (int i = ((id >> 2) & 3) + 4 * w; i > 0; --i) __builtin_amdgcn_s_sleep(BST);
  const float cs = 0.25f * 1.4426950408889634f;
  for (int t = id * 4 + w; t < 1024; t += NB * 4) {
    const int s = t >> 4, pr = t & 15;
    v4u K[18], V[2][9], q[2];
#pragma unroll
    for (int u = 0; u < 2; ++u) q[u] = ldw(rw, lane * 16 + u * 1024, WS_QB + (unsigned)(s * 16 + pr) * 2048u);
#pragma unroll
    for (int j = 0; j < 9; ++j) {
      const int sj = max(s - 8 + j, 0);
      const unsigned so = (unsigned)(sj * 16 + pr) * 2048u;
      K[2 * j] = ldw(rw, lane * 16, WS_KB + so); K[2 * j + 1] = ldw(rw, lane * 16 + 1024, WS_KB + so);
      V[0][j] = ldw(rw, lane * 16, WS_VB + so); V[1][j] = ldw(rw, lane * 16 + 1024, WS_VB + so);
    }
#pragma unroll
    for (int qt = 0; qt < 2; ++qt) {
      v4u of;
#pragma unroll
      for (int hh = 0; hh < 2; ++hh) {
        const bool mine = (g >> 1) == hh;
        const v4u qz = {mine ? q[qt].x : 0u, mine ? q[qt].y : 0u, mine ? q[qt].z : 0u, mine ? q[qt].w : 0u};
        v4f S[18];
        float m = -INFINITY;
#pragma unroll
        for (int j = 0; j < 18; ++j) {
          if ((qt == 0 && j == 17) || (qt == 1 && j == 0)) { S[j] = (v4f){-INFINITY, -INFINITY, -INFINITY, -INFINITY}; continue; }
          S[j] = mf(K[j], qz, (v4f){0, 0, 0, 0});
#pragma unroll
          for (int r = 0; r < 4; ++r) {
            if (j == qt) S[j][r] = (4 * g + r <= n) ? -INFINITY : S[j][r];
            if (j == 16 + qt) S[j][r] = (4 * g + r > n) ? -INFINITY : S[j][r];
          }
        }
#pragma unroll
        for (int j = 0; j < 9; ++j) {
          const float mj = fmaxf(fmaxf(fmaxf(S[2 * j][0], S[2 * j][1]), fmaxf(S[2 * j][2], S[2 * j][3])),
                                 fmaxf(fmaxf(S[2 * j + 1][0], S[2 * j + 1][1]), fmaxf(S[2 * j + 1][2], S[2 * j + 1][3])));
          m = (s - 8 + j < 0) ? m : fmaxf(m, mj);
        }
        m = xmax(m);
        const float mc = m * cs;
        v2f l2 = {0.f, 0.f};
        v4f o = {0, 0, 0, 0};
#pragma unroll
        for (int j = 0; j < 9; ++j) {
          const float bj = (s - 8 + j < 0) ? -INFINITY : -mc;
          float e[8];
#pragma unroll
          for (int r = 0; r < 8; ++r) e[r] = __builtin_amdgcn_exp2f(fmaf(S[2 * j + (r >> 2)][r & 3], cs, bj));
          l2 += ((v2f){e[0], e[1]} + (v2f){e[2], e[3]}) + ((v2f){e[4], e[5]} + (v2f){e[6], e[7]});
          o = mf(V[hh][j], (v4u){pk(e[0], e[1]), pk(e[2], e[3]), pk(e[4], e[5]), pk(e[6], e[7])}, o);
        }
        const float l = l2.x + l2.y;
        const float rl = __builtin_amdgcn_rcpf(xsum(l));
        of[2 * hh] = pk(o[0] * rl, o[1] * rl); of[2 * hh + 1] = pk(o[2] * rl, o[3] * rl);
      }
      stw(of, rw, lane * 16, WS_OB + (unsigned)((s * 2 + qt) * 16 + pr) * 1024u);
    }
    drain();
    if (lane == 0) __hip_atomic_fetch_add(ctl(ws, C_OC + s * 128), 1u, RLX);
    TSB(4 + t / (NB * 4));
  }
}

DI void role_d(const Params& p, char* ws, LAS char* lds, int set, int kh, int c, int w) {
  const rsrc_t rw = __builtin_amdgcn_make_buffer_rsrc(ws, 0, WS_END, 0x00020000);
  const int lane = lane_id(), n = lane & 15, g = lane >> 4;
  v4u B[16][4], C[2][4];
  {
    const rsrc_t ra = __builtin_amdgcn_make_buffer_rsrc((void*)p.Wo, 0, 4096 * 1024, 0x00020000);
    const unsigned vo = (unsigned)(32 * g) * 1024u + 8 * n, base = (unsigned)(kh * 2048 + w * 512) * 1024u + 128 * c;
#pragma unroll
    for (int kb = 0; kb < 16; kb += 4) {
      v2u R[4][8];
#pragma unroll
      for (int k = kb; k < kb + 4; ++k) {
        const int kr = (k + 2 * c) & 15;
        unsigned so = base + (unsigned)((kr >> 2) * 128 + 2 * (kr & 3)) * 1024u;
        asm volatile("" : "+s"(so));
#pragma unroll
        for (int q = 0; q < 8; ++q)
          R[k - kb][q] = __builtin_amdgcn_raw_buffer_load_b64(ra, vo + (16 * ((q >> 1) & 1) + (q >> 2) + 8 * (q & 1)) * 1024u, so, 0);
      }
      __builtin_amdgcn_sched_barrier(0);
#pragma unroll
      for (int k = kb; k < kb + 4; ++k) {
#pragma unroll
        for (int q = 0; q < 4; ++q) {
          const v2u a = R[k - kb][2 * q], b = R[k - kb][2 * q + 1];
          B[k][0][q] = plo(b.x, a.x); B[k][1][q] = phi(b.x, a.x); B[k][2][q] = plo(b.y, a.y); B[k][3][q] = phi(b.y, a.y);
        }
        asm volatile("" : "+a"(B[k][0]), "+a"(B[k][1]), "+a"(B[k][2]), "+a"(B[k][3]));
      }
      __builtin_amdgcn_sched_barrier(0);
    }
    const rsrc_t rb = __builtin_amdgcn_make_buffer_rsrc((void*)p.wo, 0, 512 * 1024, 0x00020000);
    const unsigned vb = (unsigned)(4 * g) * 1024u + 8 * n, bb = (unsigned)(kh * 256 + w * 64) * 1024u + 128 * c;
    v2u R[16];
#pragma unroll
    for (int i = 0; i < 16; ++i)
      R[i] = __builtin_amdgcn_raw_buffer_load_b64(rb, vb + (32 * (i >> 3) + 16 * ((i >> 2) & 1) + (i & 3)) * 1024u, bb, 0);
    __builtin_amdgcn_sched_barrier(0);
#pragma unroll
    for (int i = 0; i < 8; ++i) {
      const v2u a = R[2 * i], b = R[2 * i + 1];
      C[i >> 2][0][i & 3] = plo(b.x, a.x); C[i >> 2][1][i & 3] = phi(b.x, a.x); C[i >> 2][2][i & 3] = plo(b.y, a.y); C[i >> 2][3][i & 3] = phi(b.y, a.y);
    }
  }
  TSD(1);
  const int rt = w >> 1, rh = w & 1;
  const bool keep = rt == kh;
  const unsigned myflag = C_XF + (unsigned)((set * 2 + kh) * 8 + c) * 128u, pflag = myflag ^ (8u * 128u);
  if (keep) {
    for (int s = set; s < 64; s += ND) {
      const unsigned xo = WS_XB + (unsigned)(((s * 8 + c) * 2 + kh) * 2 + rh) * 2048u;
      stw((v4u){~0u, ~0u, ~0u, ~0u}, rw, lane * 16, xo); stw((v4u){~0u, ~0u, ~0u, ~0u}, rw, lane * 16 + 1024, xo);
    }
  }
  drain(); bar();
  if (w == 0 && lane == 0) __hip_atomic_store(ctl(ws, myflag), 1u, RLX);
  if (!wait_ge(ws, pflag, 1)) return;
  unsigned dts[6] = {0, 0, 0, 0, 0, 0};
#define DT(i) do {} while (0)
#define DTST(a, b) do { (void)(a); (void)(b); } while (0)
  v4f k0 = {0, 0, 0, 0}, k1 = k0;
  unsigned oc = 0;
  for (int s = set;; s += ND) {
    v4f a[2][4], g0, g1;
    LAS char* pw = lds + w * 8192 + lane * 16;
    const unsigned xi = WS_XB + (unsigned)((((s - ND) * 8 + c) * 2 + kh) * 2 + rh) * 2048u;
    v4u z0 = {~0u, ~0u, ~0u, ~0u}, z1 = z0;
    if (s < 64) {
      if (oc < 52 && !wait_ge(ws, C_OC + s * 128, 52)) return;
      bar();
      TSD(2 + s / ND);
      const unsigned o0 = WS_OA + (unsigned)(s * 256 + kh * 64 + w * 16) * 1024u, p0 = WS_OB + (unsigned)(s * 32 + kh * 8 + 2 * w) * 1024u;
      v4u Y0[2], Y1[2], X0[8], X1[8];
      v4f b[2][4];
      unsigned kr = 2 * c * 1024;
#define D_LOAD(j) do { X0[j] = ldw(rw, lane * 16, o0 + kr); X1[j] = ldw(rw, lane * 16, o0 + kr + 128 * 1024); \
        kr = (kr + 1024) & 15360; asm volatile("" : "+s"(kr)); } while (0)
#pragma unroll
      for (int k = 0; k < 8; ++k) D_LOAD(k);
      __builtin_amdgcn_sched_barrier(0);
      DT(0);
#pragma unroll
      for (int k = 0; k < 16; ++k) {
        if (k == 1) DT(1);
        if (k == 8) DT(2);
#pragma unroll
        for (int t = 0; t < 4; ++t) {
          if (k == 0) { MF0_WB(a[0][t], X0[k & 7], B[k][t]); MF0_WB(a[1][t], X1[k & 7], B[k][t]); }
          else { MF_WB(a[0][t], X0[k & 7], B[k][t]); MF_WB(a[1][t], X1[k & 7], B[k][t]); }
        }
        __builtin_amdgcn_sched_barrier(0);
        if (k < 8) D_LOAD(k);
        else if (k < 10) { Y0[k - 8] = ldw(rw, lane * 16 + (k - 8) * 1024, p0); Y1[k - 8] = ldw(rw, lane * 16 + (k - 8) * 1024, p0 + 16384); }
        else if (k == 10) {
          g0 = BC(v4f, ldw(rw, (g >> 1) * 128 + (g & 1) * 16, WS_G + (unsigned)s * 512u));
          g1 = BC(v4f, ldw(rw, 256 + (g >> 1) * 128 + (g & 1) * 16, WS_G + (unsigned)s * 512u));
          oc = __hip_atomic_load(ctl(ws, C_OC + min(s + ND, 63) * 128), RLX);
        } else if (k == 11) {
          if (keep && s >= ND) { z0 = ldw(rw, lane * 16, xi); z1 = ldw(rw, lane * 16 + 1024, xi); }
        }
        __builtin_amdgcn_sched_barrier(0);
      }
#pragma unroll
      for (int t = 0; t < 4; ++t) { MF0_WV(b[0][t], Y0[0], C[0][t]); MF0_WV(b[1][t], Y1[0], C[0][t]); }
#pragma unroll
      for (int t = 0; t < 4; ++t) { MF_WV(b[0][t], Y0[1], C[1][t]); MF_WV(b[1][t], Y1[1], C[1][t]); }
      asm volatile("s_nop 7" : "+v"(b[0][0]), "+v"(b[0][1]), "+v"(b[0][2]), "+v"(b[0][3]), "+v"(b[1][0]), "+v"(b[1][1]), "+v"(b[1][2]), "+v"(b[1][3]),
                   "+v"(a[0][0]), "+v"(a[0][1]), "+v"(a[0][2]), "+v"(a[0][3]), "+v"(a[1][0]), "+v"(a[1][1]), "+v"(a[1][2]), "+v"(a[1][3]));
      oc = __builtin_amdgcn_readfirstlane(oc);
#pragma unroll
      for (int r = 0; r < 4; ++r) {
        v4f u0, u1;
#pragma unroll
        for (int t = 0; t < 4; ++t) {
          u0[t] = fmaf(g0[r], a[0][t][r] - b[0][t][r], b[0][t][r]);
          u1[t] = fmaf(g1[r], a[1][t][r] - b[1][t][r], b[1][t][r]);
        }
        *(LAS v4f*)(pw + r * 1024) = u0;
        *(LAS v4f*)(pw + 4096 + r * 1024) = u1;
      }
    }
    DT(3);
    if (keep && s >= ND) {
      const int sp = s - ND;
      for (unsigned it = 0;; ++it) {
        if (__all(max(max(max(z0.x, z0.y), max(z0.z, z0.w)), max(max(z1.x, z1.y), max(z1.z, z1.w))) != ~0u)) break;
        if (it > SPIN) { __hip_atomic_store(ctl(ws, C_ABORT), 1u, RLX); return; }
        asm volatile("" ::: "memory");
        z0 = ldw(rw, lane * 16, xi); z1 = ldw(rw, lane * 16 + 1024, xi);
      }
      const v4f y0 = BC(v4f, z0) + k0, y1 = BC(v4f, z1) + k1;
      v2u* dst = (v2u*)(p.out + (size_t)(32 * sp + 16 * rt + 4 * g + 2 * rh) * 512 + 64 * c + 4 * n);
      dst[0] = (v2u){pk(y0[0], y0[1]), pk(y0[2], y0[3])};
      dst[128] = (v2u){pk(y1[0], y1[1]), pk(y1[2], y1[3])};
      TSD(40 + sp / ND);
    }
    DT(4);
    if (s >= 64) break;
    bar();
    k0 = k1 = (v4f){0, 0, 0, 0};
#pragma unroll
    for (int u = 0; u < 4; ++u) {
      k0 += *(LAS v4f*)(lds + u * 8192 + rt * 4096 + (2 * rh) * 1024 + lane * 16);
      k1 += *(LAS v4f*)(lds + u * 8192 + rt * 4096 + (2 * rh + 1) * 1024 + lane * 16);
    }
    DT(5);
    if (s / ND == 12) { for (int i = 0; i < 6; ++i) { unsigned vo_ = w * 512 + (110 + i) * 4; unsigned tv_ = dts[i]; DTST(vo_, tv_); } }
    if (!keep) {
      const unsigned xo = WS_XB + (unsigned)(((s * 8 + c) * 2 + rt) * 2 + rh) * 2048u;
      v4u u0 = BC(v4u, k0), u1 = BC(v4u, k1);
#pragma unroll
      for (int t = 0; t < 4; ++t) { u0[t] = min(u0[t], 0xfffffffeu); u1[t] = min(u1[t], 0xfffffffeu); }
      stw(u0, rw, lane * 16, xo); stw(u1, rw, lane * 16 + 1024, xo);
    }
  }
}

DI void warm(const void* W, unsigned bytes, unsigned vo, unsigned so, unsigned step, int n, bool wait) {
  const rsrc_t r = __builtin_amdgcn_make_buffer_rsrc((void*)W, 0, bytes, 0x00020000);
  for (int i = 0; i < n; ++i) {
    unsigned k;
    asm volatile("s_mov_b32 %0, m0\n s_mov_b32 m0, 0\n s_nop 0\n buffer_load_dwordx4 %1, %2, %3 offen lds\n s_mov_b32 m0, %0"
                 : "=&s"(k) : "v"(vo), "s"(r), "s"(so + i * step) : "memory");
  }
  if (wait) drain();
}
#define CODE_WARM(lab) do { unsigned long long pc_; unsigned len_; \
  asm volatile("s_getpc_b64 %0\n1:\n s_mov_b32 %1, " lab "-1b" : "=s"(pc_), "=s"(len_)); \
  if ((int)len_ > 0) { const rsrc_t rc_ = __builtin_amdgcn_make_buffer_rsrc((void*)pc_, 0, len_, 0x00020000); \
    for (unsigned o_ = w * 4096; o_ < len_; o_ += 16384) { unsigned k_;   \
      asm volatile("s_mov_b32 %0, m0\n s_mov_b32 m0, 0\n s_nop 0\n buffer_load_dword %1, %2, %3 offen lds\n s_mov_b32 m0, %0" \
                   : "=&s"(k_) : "v"(lane_id() * 64), "s"(rc_), "s"(o_) : "memory"); } } } while (0)
#define CODE_END(lab) asm volatile(lab ":")
DI void probe_sel(char* ws, u64 tag) {
  const int lane = lane_id();
  const rsrc_t rw = __builtin_amdgcn_make_buffer_rsrc(ws, 0, WS_END, 0x00020000);
  const unsigned base = WS_COMM + 0x1E0000;
  __builtin_amdgcn_raw_buffer_store_b32(0u, rw, (lane & 31) * 256 + (lane >> 5) * 8192, base, 16);
  drain();
  unsigned best[2] = {~0u, ~0u};
#pragma unroll
  for (int i = 0; i < 8; ++i) {
    const int hf = i & 1;
    const unsigned long long t0 = __builtin_amdgcn_s_memtime();
    asm volatile("" ::: "memory");
    unsigned v = __builtin_amdgcn_raw_buffer_load_b32(rw, (lane & 7) * 256, base + hf * 8192 + (i >> 1) * 2048, 16);
    asm volatile("s_waitcnt vmcnt(0)" : "+v"(v)::"memory");
    const unsigned dt = (unsigned)(__builtin_amdgcn_s_memtime() - t0);
    best[hf] = min(best[hf], dt);
  }
  if (wait_z(ws, tag) && lane == 0) __hip_atomic_store(ctl64(ws, C_SELW), (tag << 1) | (best[1] < best[0] ? 1u : 0u), RLX);
}

#define CHAIN_CLOB "v0","v1","v2","v3","v4","v5","v6","v7","v8","v9","v10","v11","v12","v13","v14","v15","v16","v17","v18","v19","v20","v21","v22","v23","v24","v25","v26","v27","v28","v29","v30","v31","v32","v33","v34","v35","v36","v37","v38","v39","v40","v41","v42","v43","v44","v45","v46","v47","v48","v49","v50","v51","v52","v53","v54","v55","v56","v57","v58","v59","v60","v61","v62","v63","v64","v65","v66","v67","v68","v69","v70","v71","v72","v73","v74","v75","v76","v77","v78","v79","v80","v81","v82","v83","v84","v85","v86","v87","v88","v89","v90","v91","v92","v93","v94","v95","v96","v97","v98","v99","v100","v101","v102","v103","v104","v105","v106","v107","v108","v109","v110","v111","v112","v113","v114","v115","v116","v117","v118","v119","v120","v121","v122","v123","v124","v125","v126","v127","v128","v129","v130","v131","v132","v133","v134","v135","v136","v137","v138","v139","v140","v141","v142","v143","v144","v145","v146","v147","v148","v149","v150","v151","v152","v153","v154","v155","v156","v157","v158","v159","v160","v161","v162","v163","v164","v165","v166","v167","v168","v169","v170","v171","v172","v173","v174","v175","v176","v177","v178","v179","v180","v181","v182","v183","v184","v185","v186","v187","v188","v189","v190","v191","v192","v193","v194","v195","v196","v197","v198","v199","v200","v201","v202","v203","v204","v205","v206","v207","v208","v209","v210","v211","v212","v213","v214","v215","v216","v217","v218","v219","v220","v221","v222","v223","v224","v225","v226","v227","v228","v229","v230","v231","v232","v233","v234","v235","v236","v237","v238","v239","v240","v241","v242","v243","v244","v245","v246","v247","v248","v249","v250","v251","v252","v253","v254","v255","a0","a1","a2","a3","a4","a5","a6","a7","a8","a9","a10","a11","a12","a13","a14","a15","a16","a17","a18","a19","a20","a21","a22","a23","a24","a25","a26","a27","a28","a29","a30","a31","a32","a33","a34","a35","a36","a37","a38","a39","a40","a41","a42","a43","a44","a45","a46","a47","a48","a49","a50","a51","a52","a53","a54","a55","a56","a57","a58","a59","a60","a61","a62","a63","a64","a65","a66","a67","a68","a69","a70","a71","a72","a73","a74","a75","a76","a77","a78","a79","a80","a81","a82","a83","a84","a85","a86","a87","a88","a89","a90","a91","a92","a93","a94","a95","a96","a97","a98","a99","a100","a101","a102","a103","a104","a105","a106","a107","a108","a109","a110","a111","a112","a113","a114","a115","a116","a117","a118","a119","a120","a121","a122","a123","a124","a125","a126","a127","a128","a129","a130","a131","a132","a133","a134","a135","a136","a137","a138","a139","a140","a141","a142","a143","a144","a145","a146","a147","a148","a149","a150","a151","a152","a153","a154","a155","a156","a157","a158","a159","a160","a161","a162","a163","a164","a165","a166","a167","a168","a169","a170","a171","a172","a173","a174","a175","a176","a177","a178","a179","a180","a181","a182","a183","a184","a185","a186","a187","a188","a189","a190","a191","a192","a193","a194","a195","a196","a197","a198","a199","a200","a201","a202","a203","a204","a205","a206","a207","a208","a209","a210","a211","a212","a213","a214","a215","a216","a217","a218","a219","a220","a221","a222","a223","a224","a225","a226","a227","a228","a229","a230","a231","a232","a233","a234","a235","a236","a237","a238","a239","a240","a241","a242","a243","a244","a245","a246","a247","a248","a249","a250","a251","a252","a253","a254","a255","s26","s27","s28","s29","s30","s31","s34","s35","s36","s37","s38","s39","s40","s41","s42","s43","s44","s45","s46","s47","s48","s49","s50","s51","s52","s53","s54","s55","s56","s57","s58","s59","s60","s61","s62","s63","s64","s65","s66","s67","s68","s69","s70","s71","s72","s73","s74","s75","s76","s77","s78","s79","s80","s81","s82","s83","s84","s85","s86","s87","s88","s89","s90","s91","s92","s93","s94","s95","s96","s97","s98","s99","vcc","scc","memory"
#define PLACE 1
#define CL(r) "Lmega_end_" r
typedef const __attribute__((address_space(4))) Params* kptr;
DI Params get_params(kptr k) {
  Params p;
  p.x = k->x; p.mem0 = k->mem0; p.Wq = k->Wq; p.Wk = k->Wk; p.Wv = k->Wv; p.Wo = k->Wo; p.wqkv = k->wqkv; p.wo = k->wo; p.gw = k->gw;
  p.out = k->out; p.ws = k->ws; p.P1 = k->P1; p.P2 = k->P2; p.da = k->da; p.db = k->db; p.dd = k->dd; p.pad = k->pad;
  return p;
}
template <int V, int PASS>
DI unsigned run_role(kptr kp, LAS char* lds, int b, int w, u64& tag) {
  const int x = b & 7, y = b >> 3, slot = PASS == 0 ? b : -1;
  unsigned lost = 0;
  if (x >= 4 && y < 24) {
    asm volatile("" : "+s"(kp));
    const Params p = get_params(kp);
    char* ws = p.ws;
    const u64 ev = PASS == 0 ? __hip_atomic_load(ctl64(ws, C_E), RLX) : tag - 1;
    if (PASS == 0 && (p.pad & 4)) CODE_WARM(CL("a"));
    if (y >= 16) for (int i = 0; i < p.da; ++i) __builtin_amdgcn_s_sleep(16);
    lost = role_a(p, ws, lds, (x - 4) * 8 + (y & 7), y >> 3, w, slot, ev, tag);
    if (PASS == 0) CODE_END(CL("a"));
  } else if (x < 4 && y < 16) {
    const int role = y >> 3, h = (y & 7) * 4 + x;
  if constexpr (V==0) { if (role==0) { asm volatile(
"s_load_dwordx8 s[52:59], %2, 16\n"
"s_load_dwordx2 s[30:31], %2, 8\n"
"s_load_dwordx2 s[40:41], %2, 80\n"
"s_load_dwordx2 s[88:89], %2, 88\n"
"s_mov_b32 s42, 79691776\n"
"s_mov_b32 s43, 0x20000\n"
"s_mov_b32 s47, 0x20000\n"
"s_mov_b32 %0, 0\n"
"s_mov_b32 s48, %3\n"
"s_mov_b32 s49, %4\n"
"s_mov_b32 s70, 70\n"
"s_mov_b32 s71, 50\n"
"s_mov_b32 s51, 63\n"
"s_mov_b32 s84, 1\n"
"s_mov_b32 s78, 0\n"
"s_mov_b32 s50, 0\n"
"s_mov_b32 s76, 0\n"
"s_mov_b32 s77, 0\n"
"s_mov_b32 s86, 0\n"
"s_mov_b32 s29, 0\n"
"s_mov_b32 s72, 0\n"
"s_mov_b32 s95, 0\n"
"s_mov_b32 s62, 0x05040100\n"
"s_mov_b32 s63, 0x07060302\n"
"s_mov_b32 s64, 0x3e0293ee\n"
"s_mov_b32 s65, 0x3e0293ee\n"
"v_mbcnt_lo_u32_b32 v15, -1, 0\n"
"v_mbcnt_hi_u32_b32 v15, -1, v15\n"
"v_lshlrev_b32 v0, 4, v15\n"
"v_and_b32 v24, 15, v15\n"
"v_lshrrev_b32 v25, 4, v15\n"
"v_lshrrev_b32 v26, 1, v25\n"
"v_and_b32 v27, 1, v25\n"
"v_lshlrev_b32 v28, 18, v26\n"
"v_lshlrev_b32 v29, 16, v27\n"
"v_add_u32 v28, v28, v29\n"
"v_lshlrev_b32 v29, 4, v24\n"
"v_add_u32 v28, v28, v29\n"
"s_lshl_b32 s73, s48, 8\n"
"v_add_u32 v28, s73, v28\n"
"s_lshl_b32 s85, s49, 20\n"
"s_lshl_b32 s87, s49, 11\n"
"s_waitcnt lgkmcnt(0)\n"
"v_mov_b32 v22, 0\n"
"s_mov_b32 s26, 38400\n"
"buffer_load_dwordx2 v[20:21], v22, s[40:43], s26 offen sc1\n"
"s_getpc_b64 s[44:45]\n"
"LH_PC%=:\n"
"s_mov_b32 s46, LH_END%=-LH_PC%=\n"
"v_lshlrev_b32 v31, 6, v15\n"
"s_lshl_b32 s73, s49, 12\n"
"buffer_load_dword v23, v31, s[44:47], s73 offen\n"
"v_lshlrev_b32 v30, 16, v26\n"
"v_lshlrev_b32 v31, 10, v27\n"
"v_add_u32 v30, v30, v31\n"
"v_add_u32 v30, v30, v29\n"
"s_lshl_b32 s73, s48, 17\n"
"s_lshl_b32 s74, s49, 8\n"
"s_add_u32 s73, s73, s74\n"
"v_add_u32 v30, s73, v30\n"
"s_mov_b64 s[44:45], s[54:55]\n"
"s_mov_b32 s46, 4194304\n"
"s_add_u32 s73, s85, 0\n"
"buffer_load_dwordx4 v[128:131], v28, s[44:47], s73 offen\n"
"s_add_u32 s73, s85, 8192\n"
"buffer_load_dwordx4 v[132:135], v28, s[44:47], s73 offen\n"
"s_add_u32 s73, s85, 131072\n"
"buffer_load_dwordx4 v[136:139], v28, s[44:47], s73 offen\n"
"s_add_u32 s73, s85, 139264\n"
"buffer_load_dwordx4 v[140:143], v28, s[44:47], s73 offen\n"
"s_add_u32 s73, s85, 16384\n"
"buffer_load_dwordx4 v[144:147], v28, s[44:47], s73 offen\n"
"s_add_u32 s73, s85, 24576\n"
"buffer_load_dwordx4 v[148:151], v28, s[44:47], s73 offen\n"
"s_add_u32 s73, s85, 147456\n"
"buffer_load_dwordx4 v[152:155], v28, s[44:47], s73 offen\n"
"s_add_u32 s73, s85, 155648\n"
"buffer_load_dwordx4 v[156:159], v28, s[44:47], s73 offen\n"
"s_add_u32 s73, s85, 524288\n"
"buffer_load_dwordx4 v[160:163], v28, s[44:47], s73 offen\n"
"s_add_u32 s73, s85, 532480\n"
"buffer_load_dwordx4 v[164:167], v28, s[44:47], s73 offen\n"
"s_add_u32 s73, s85, 655360\n"
"buffer_load_dwordx4 v[168:171], v28, s[44:47], s73 offen\n"
"s_add_u32 s73, s85, 663552\n"
"buffer_load_dwordx4 v[172:175], v28, s[44:47], s73 offen\n"
"s_add_u32 s73, s85, 540672\n"
"buffer_load_dwordx4 v[176:179], v28, s[44:47], s73 offen\n"
"s_add_u32 s73, s85, 548864\n"
"buffer_load_dwordx4 v[180:183], v28, s[44:47], s73 offen\n"
"s_add_u32 s73, s85, 671744\n"
"buffer_load_dwordx4 v[184:187], v28, s[44:47], s73 offen\n"
"s_add_u32 s73, s85, 679936\n"
"buffer_load_dwordx4 v[188:191], v28, s[44:47], s73 offen\n"
"s_add_u32 s73, s85, 32768\n"
"buffer_load_dwordx4 v[192:195], v28, s[44:47], s73 offen\n"
"s_add_u32 s73, s85, 40960\n"
"buffer_load_dwordx4 v[196:199], v28, s[44:47], s73 offen\n"
"s_add_u32 s73, s85, 163840\n"
"buffer_load_dwordx4 v[200:203], v28, s[44:47], s73 offen\n"
"s_add_u32 s73, s85, 172032\n"
"buffer_load_dwordx4 v[204:207], v28, s[44:47], s73 offen\n"
"s_add_u32 s73, s85, 49152\n"
"buffer_load_dwordx4 v[208:211], v28, s[44:47], s73 offen\n"
"s_add_u32 s73, s85, 57344\n"
"buffer_load_dwordx4 v[212:215], v28, s[44:47], s73 offen\n"
"s_add_u32 s73, s85, 180224\n"
"buffer_load_dwordx4 v[216:219], v28, s[44:47], s73 offen\n"
"s_add_u32 s73, s85, 188416\n"
"buffer_load_dwordx4 v[220:223], v28, s[44:47], s73 offen\n"
"s_add_u32 s73, s85, 557056\n"
"buffer_load_dwordx4 v[224:227], v28, s[44:47], s73 offen\n"
"s_add_u32 s73, s85, 565248\n"
"buffer_load_dwordx4 v[228:231], v28, s[44:47], s73 offen\n"
"s_add_u32 s73, s85, 688128\n"
"buffer_load_dwordx4 v[232:235], v28, s[44:47], s73 offen\n"
"s_add_u32 s73, s85, 696320\n"
"buffer_load_dwordx4 v[236:239], v28, s[44:47], s73 offen\n"
"s_add_u32 s73, s85, 573440\n"
"buffer_load_dwordx4 v[240:243], v28, s[44:47], s73 offen\n"
"s_add_u32 s73, s85, 581632\n"
"buffer_load_dwordx4 v[244:247], v28, s[44:47], s73 offen\n"
"s_add_u32 s73, s85, 704512\n"
"buffer_load_dwordx4 v[248:251], v28, s[44:47], s73 offen\n"
"s_add_u32 s73, s85, 712704\n"
"buffer_load_dwordx4 v[252:255], v28, s[44:47], s73 offen\n"
"s_waitcnt vmcnt(33)\n"
"v_readfirstlane_b32 s66, v20\n"
"v_readfirstlane_b32 s67, v21\n"
"s_cmp_lt_i32 %5, 0\n"
"s_cbranch_scc1 LH_CT1%=\n"
"s_add_u32 s66, s66, 1\n"
"s_addc_u32 s67, s67, 0\n"
"s_bitset0_b32 s67, 31\n"
"s_mov_b64 %1, s[66:67]\n"
"LH_CT1%=:\n"
"s_mov_b64 s[66:67], %1\n"
"s_lshl_b64 s[68:69], s[66:67], 1\n"
"s_or_b32 s68, s68, 1\n"
"v_mov_b32 v20, s66\n"
"v_mov_b32 v21, s67\n"
"v_not_b32 v18, v20\n"
"v_mov_b32 v19, 0\n"
"s_lshl_b32 s73, %5, 3\n"
"s_add_u32 s73, s73, 38912\n"
"s_lshr_b32 s74, %5, 31\n"
"s_or_b32 s74, s74, s49\n"
"s_cselect_b32 s74, 0x80000000, 0\n"
"s_cselect_b32 s73, 0, s73\n"
"v_mov_b32 v17, s74\n"
"s_mov_b64 exec, 1\n"
"buffer_atomic_swap_x2 v[20:21], v17, s[40:43], s73 offen sc0 sc1\n"
"s_mov_b64 exec, -1\n"
"s_sub_u32 s73, s49, 0\n"
"s_lshl_b32 s73, s73, 1\n"
"s_and_b32 s73, s73, 7\n"
"s_lshl_b32 s73, s73, 12\n"
"s_add_u32 s73, s73, 0\n"
"v_add_u32 v32, s73, v0\n"
"s_sub_u32 s73, s49, 1\n"
"s_lshl_b32 s73, s73, 1\n"
"s_and_b32 s73, s73, 7\n"
"s_lshl_b32 s73, s73, 12\n"
"s_add_u32 s73, s73, 32768\n"
"v_add_u32 v33, s73, v0\n"
"s_sub_u32 s73, s49, 2\n"
"s_lshl_b32 s73, s73, 1\n"
"s_and_b32 s73, s73, 7\n"
"s_lshl_b32 s73, s73, 12\n"
"s_add_u32 s73, s73, 65536\n"
"v_add_u32 v34, s73, v0\n"
"s_sub_u32 s73, s49, 3\n"
"s_lshl_b32 s73, s73, 1\n"
"s_and_b32 s73, s73, 7\n"
"s_lshl_b32 s73, s73, 12\n"
"s_add_u32 s73, s73, 98304\n"
"v_add_u32 v35, s73, v0\n"
"s_lshl_b32 s73, s49, 15\n"
"v_add_u32 v2, s73, v0\n"
"s_waitcnt vmcnt(31)\n"
"v_perm_b32 v40, v132, v128, s62\n"
"v_perm_b32 v44, v132, v128, s63\n"
"v_perm_b32 v48, v133, v129, s62\n"
"v_perm_b32 v52, v133, v129, s63\n"
"v_perm_b32 v56, v134, v130, s62\n"
"v_perm_b32 v60, v134, v130, s63\n"
"v_perm_b32 v64, v135, v131, s62\n"
"v_perm_b32 v68, v135, v131, s63\n"
"s_waitcnt vmcnt(29)\n"
"v_perm_b32 v41, v140, v136, s62\n"
"v_perm_b32 v45, v140, v136, s63\n"
"v_perm_b32 v49, v141, v137, s62\n"
"v_perm_b32 v53, v141, v137, s63\n"
"v_perm_b32 v57, v142, v138, s62\n"
"v_perm_b32 v61, v142, v138, s63\n"
"v_perm_b32 v65, v143, v139, s62\n"
"v_perm_b32 v69, v143, v139, s63\n"
"s_waitcnt vmcnt(27)\n"
"v_perm_b32 v42, v148, v144, s62\n"
"v_perm_b32 v46, v148, v144, s63\n"
"v_perm_b32 v50, v149, v145, s62\n"
"v_perm_b32 v54, v149, v145, s63\n"
"v_perm_b32 v58, v150, v146, s62\n"
"v_perm_b32 v62, v150, v146, s63\n"
"v_perm_b32 v66, v151, v147, s62\n"
"v_perm_b32 v70, v151, v147, s63\n"
"s_waitcnt vmcnt(25)\n"
"v_perm_b32 v43, v156, v152, s62\n"
"v_perm_b32 v47, v156, v152, s63\n"
"v_perm_b32 v51, v157, v153, s62\n"
"v_perm_b32 v55, v157, v153, s63\n"
"v_perm_b32 v59, v158, v154, s62\n"
"v_perm_b32 v63, v158, v154, s63\n"
"v_perm_b32 v67, v159, v155, s62\n"
"v_perm_b32 v71, v159, v155, s63\n"
"ds_write_b128 v32, v[40:43] offset:0\n"
"ds_write_b128 v32, v[44:47] offset:2048\n"
"ds_write_b128 v33, v[48:51] offset:0\n"
"ds_write_b128 v33, v[52:55] offset:2048\n"
"ds_write_b128 v34, v[56:59] offset:0\n"
"ds_write_b128 v34, v[60:63] offset:2048\n"
"ds_write_b128 v35, v[64:67] offset:0\n"
"ds_write_b128 v35, v[68:71] offset:2048\n"
"s_mov_b64 s[44:45], s[52:53]\n"
"s_mov_b32 s46, 4194304\n"
"s_add_u32 s73, s85, 0\n"
"buffer_load_dwordx4 a[0:3], v28, s[44:47], s73 offen\n"
"s_add_u32 s73, s85, 8192\n"
"buffer_load_dwordx4 a[4:7], v28, s[44:47], s73 offen\n"
"s_add_u32 s73, s85, 131072\n"
"buffer_load_dwordx4 a[8:11], v28, s[44:47], s73 offen\n"
"s_add_u32 s73, s85, 139264\n"
"buffer_load_dwordx4 a[12:15], v28, s[44:47], s73 offen\n"
"s_add_u32 s73, s85, 16384\n"
"buffer_load_dwordx4 a[16:19], v28, s[44:47], s73 offen\n"
"s_add_u32 s73, s85, 24576\n"
"buffer_load_dwordx4 a[20:23], v28, s[44:47], s73 offen\n"
"s_add_u32 s73, s85, 147456\n"
"buffer_load_dwordx4 a[24:27], v28, s[44:47], s73 offen\n"
"s_add_u32 s73, s85, 155648\n"
"buffer_load_dwordx4 a[28:31], v28, s[44:47], s73 offen\n"
"s_add_u32 s73, s85, 524288\n"
"buffer_load_dwordx4 a[32:35], v28, s[44:47], s73 offen\n"
"s_add_u32 s73, s85, 532480\n"
"buffer_load_dwordx4 a[36:39], v28, s[44:47], s73 offen\n"
"s_add_u32 s73, s85, 655360\n"
"buffer_load_dwordx4 a[40:43], v28, s[44:47], s73 offen\n"
"s_add_u32 s73, s85, 663552\n"
"buffer_load_dwordx4 a[44:47], v28, s[44:47], s73 offen\n"
"s_add_u32 s73, s85, 540672\n"
"buffer_load_dwordx4 a[48:51], v28, s[44:47], s73 offen\n"
"s_add_u32 s73, s85, 548864\n"
"buffer_load_dwordx4 a[52:55], v28, s[44:47], s73 offen\n"
"s_add_u32 s73, s85, 671744\n"
"buffer_load_dwordx4 a[56:59], v28, s[44:47], s73 offen\n"
"s_add_u32 s73, s85, 679936\n"
"buffer_load_dwordx4 a[60:63], v28, s[44:47], s73 offen\n"
"s_add_u32 s73, s85, 32768\n"
"buffer_load_dwordx4 a[64:67], v28, s[44:47], s73 offen\n"
"s_add_u32 s73, s85, 40960\n"
"buffer_load_dwordx4 a[68:71], v28, s[44:47], s73 offen\n"
"s_add_u32 s73, s85, 163840\n"
"buffer_load_dwordx4 a[72:75], v28, s[44:47], s73 offen\n"
"s_add_u32 s73, s85, 172032\n"
"buffer_load_dwordx4 a[76:79], v28, s[44:47], s73 offen\n"
"s_add_u32 s73, s85, 49152\n"
"buffer_load_dwordx4 a[80:83], v28, s[44:47], s73 offen\n"
"s_add_u32 s73, s85, 57344\n"
"buffer_load_dwordx4 a[84:87], v28, s[44:47], s73 offen\n"
"s_add_u32 s73, s85, 180224\n"
"buffer_load_dwordx4 a[88:91], v28, s[44:47], s73 offen\n"
"s_add_u32 s73, s85, 188416\n"
"buffer_load_dwordx4 a[92:95], v28, s[44:47], s73 offen\n"
"s_add_u32 s73, s85, 557056\n"
"buffer_load_dwordx4 a[96:99], v28, s[44:47], s73 offen\n"
"s_add_u32 s73, s85, 565248\n"
"buffer_load_dwordx4 a[100:103], v28, s[44:47], s73 offen\n"
"s_add_u32 s73, s85, 688128\n"
"buffer_load_dwordx4 a[104:107], v28, s[44:47], s73 offen\n"
"s_add_u32 s73, s85, 696320\n"
"buffer_load_dwordx4 a[108:111], v28, s[44:47], s73 offen\n"
"s_add_u32 s73, s85, 573440\n"
"buffer_load_dwordx4 a[112:115], v28, s[44:47], s73 offen\n"
"s_add_u32 s73, s85, 581632\n"
"buffer_load_dwordx4 a[116:119], v28, s[44:47], s73 offen\n"
"s_add_u32 s73, s85, 704512\n"
"buffer_load_dwordx4 a[120:123], v28, s[44:47], s73 offen\n"
"s_add_u32 s73, s85, 712704\n"
"buffer_load_dwordx4 a[124:127], v28, s[44:47], s73 offen\n"
"s_mov_b64 s[44:45], s[58:59]\n"
"s_mov_b32 s46, 4194304\n"
"s_mov_b32 s73, 0\n"
"buffer_load_dwordx4 v[80:83], v30, s[44:47], s73 offen\n"
"s_mov_b32 s73, 8192\n"
"buffer_load_dwordx4 v[84:87], v30, s[44:47], s73 offen\n"
"s_mov_b32 s73, 32768\n"
"buffer_load_dwordx4 v[88:91], v30, s[44:47], s73 offen\n"
"s_mov_b32 s73, 40960\n"
"buffer_load_dwordx4 v[92:95], v30, s[44:47], s73 offen\n"
"s_mov_b32 s73, 16384\n"
"buffer_load_dwordx4 v[96:99], v30, s[44:47], s73 offen\n"
"s_waitcnt vmcnt(60)\n"
"v_perm_b32 v40, v164, v160, s62\n"
"v_perm_b32 v44, v164, v160, s63\n"
"v_perm_b32 v48, v165, v161, s62\n"
"v_perm_b32 v52, v165, v161, s63\n"
"v_perm_b32 v56, v166, v162, s62\n"
"v_perm_b32 v60, v166, v162, s63\n"
"v_perm_b32 v64, v167, v163, s62\n"
"v_perm_b32 v68, v167, v163, s63\n"
"s_waitcnt vmcnt(58)\n"
"v_perm_b32 v41, v172, v168, s62\n"
"v_perm_b32 v45, v172, v168, s63\n"
"v_perm_b32 v49, v173, v169, s62\n"
"v_perm_b32 v53, v173, v169, s63\n"
"v_perm_b32 v57, v174, v170, s62\n"
"v_perm_b32 v61, v174, v170, s63\n"
"v_perm_b32 v65, v175, v171, s62\n"
"v_perm_b32 v69, v175, v171, s63\n"
"s_waitcnt vmcnt(56)\n"
"v_perm_b32 v42, v180, v176, s62\n"
"v_perm_b32 v46, v180, v176, s63\n"
"v_perm_b32 v50, v181, v177, s62\n"
"v_perm_b32 v54, v181, v177, s63\n"
"v_perm_b32 v58, v182, v178, s62\n"
"v_perm_b32 v62, v182, v178, s63\n"
"v_perm_b32 v66, v183, v179, s62\n"
"v_perm_b32 v70, v183, v179, s63\n"
"s_waitcnt vmcnt(54)\n"
"v_perm_b32 v43, v188, v184, s62\n"
"v_perm_b32 v47, v188, v184, s63\n"
"v_perm_b32 v51, v189, v185, s62\n"
"v_perm_b32 v55, v189, v185, s63\n"
"v_perm_b32 v59, v190, v186, s62\n"
"v_perm_b32 v63, v190, v186, s63\n"
"v_perm_b32 v67, v191, v187, s62\n"
"v_perm_b32 v71, v191, v187, s63\n"
"ds_write_b128 v32, v[40:43] offset:1024\n"
"ds_write_b128 v32, v[44:47] offset:3072\n"
"ds_write_b128 v33, v[48:51] offset:1024\n"
"ds_write_b128 v33, v[52:55] offset:3072\n"
"ds_write_b128 v34, v[56:59] offset:1024\n"
"ds_write_b128 v34, v[60:63] offset:3072\n"
"ds_write_b128 v35, v[64:67] offset:1024\n"
"ds_write_b128 v35, v[68:71] offset:3072\n"
"s_mov_b32 s73, 24576\n"
"buffer_load_dwordx4 v[100:103], v30, s[44:47], s73 offen\n"
"s_mov_b32 s73, 49152\n"
"buffer_load_dwordx4 v[104:107], v30, s[44:47], s73 offen\n"
"s_mov_b32 s73, 57344\n"
"buffer_load_dwordx4 v[108:111], v30, s[44:47], s73 offen\n"
"s_mov_b32 s73, 2048\n"
"buffer_load_dwordx4 v[112:115], v30, s[44:47], s73 offen\n"
"s_mov_b32 s73, 10240\n"
"buffer_load_dwordx4 v[116:119], v30, s[44:47], s73 offen\n"
"s_mov_b32 s73, 34816\n"
"buffer_load_dwordx4 v[120:123], v30, s[44:47], s73 offen\n"
"s_mov_b32 s73, 43008\n"
"buffer_load_dwordx4 v[124:127], v30, s[44:47], s73 offen\n"
"s_waitcnt vmcnt(59)\n"
"v_perm_b32 v40, v196, v192, s62\n"
"v_perm_b32 v44, v196, v192, s63\n"
"v_perm_b32 v48, v197, v193, s62\n"
"v_perm_b32 v52, v197, v193, s63\n"
"v_perm_b32 v56, v198, v194, s62\n"
"v_perm_b32 v60, v198, v194, s63\n"
"v_perm_b32 v64, v199, v195, s62\n"
"v_perm_b32 v68, v199, v195, s63\n"
"s_waitcnt vmcnt(57)\n"
"v_perm_b32 v41, v204, v200, s62\n"
"v_perm_b32 v45, v204, v200, s63\n"
"v_perm_b32 v49, v205, v201, s62\n"
"v_perm_b32 v53, v205, v201, s63\n"
"v_perm_b32 v57, v206, v202, s62\n"
"v_perm_b32 v61, v206, v202, s63\n"
"v_perm_b32 v65, v207, v203, s62\n"
"v_perm_b32 v69, v207, v203, s63\n"
"s_waitcnt vmcnt(55)\n"
"v_perm_b32 v42, v212, v208, s62\n"
"v_perm_b32 v46, v212, v208, s63\n"
"v_perm_b32 v50, v213, v209, s62\n"
"v_perm_b32 v54, v213, v209, s63\n"
"v_perm_b32 v58, v214, v210, s62\n"
"v_perm_b32 v62, v214, v210, s63\n"
"v_perm_b32 v66, v215, v211, s62\n"
"v_perm_b32 v70, v215, v211, s63\n"
"s_waitcnt vmcnt(53)\n"
"v_perm_b32 v43, v220, v216, s62\n"
"v_perm_b32 v47, v220, v216, s63\n"
"v_perm_b32 v51, v221, v217, s62\n"
"v_perm_b32 v55, v221, v217, s63\n"
"v_perm_b32 v59, v222, v218, s62\n"
"v_perm_b32 v63, v222, v218, s63\n"
"v_perm_b32 v67, v223, v219, s62\n"
"v_perm_b32 v71, v223, v219, s63\n"
"ds_write_b128 v32, v[40:43] offset:4096\n"
"ds_write_b128 v32, v[44:47] offset:6144\n"
"ds_write_b128 v33, v[48:51] offset:4096\n"
"ds_write_b128 v33, v[52:55] offset:6144\n"
"ds_write_b128 v34, v[56:59] offset:4096\n"
"ds_write_b128 v34, v[60:63] offset:6144\n"
"ds_write_b128 v35, v[64:67] offset:4096\n"
"ds_write_b128 v35, v[68:71] offset:6144\n"
"s_waitcnt vmcnt(51)\n"
"v_perm_b32 v40, v228, v224, s62\n"
"v_perm_b32 v44, v228, v224, s63\n"
"v_perm_b32 v48, v229, v225, s62\n"
"v_perm_b32 v52, v229, v225, s63\n"
"v_perm_b32 v56, v230, v226, s62\n"
"v_perm_b32 v60, v230, v226, s63\n"
"v_perm_b32 v64, v231, v227, s62\n"
"v_perm_b32 v68, v231, v227, s63\n"
"s_waitcnt vmcnt(49)\n"
"v_perm_b32 v41, v236, v232, s62\n"
"v_perm_b32 v45, v236, v232, s63\n"
"v_perm_b32 v49, v237, v233, s62\n"
"v_perm_b32 v53, v237, v233, s63\n"
"v_perm_b32 v57, v238, v234, s62\n"
"v_perm_b32 v61, v238, v234, s63\n"
"v_perm_b32 v65, v239, v235, s62\n"
"v_perm_b32 v69, v239, v235, s63\n"
"s_waitcnt vmcnt(47)\n"
"v_perm_b32 v42, v244, v240, s62\n"
"v_perm_b32 v46, v244, v240, s63\n"
"v_perm_b32 v50, v245, v241, s62\n"
"v_perm_b32 v54, v245, v241, s63\n"
"v_perm_b32 v58, v246, v242, s62\n"
"v_perm_b32 v62, v246, v242, s63\n"
"v_perm_b32 v66, v247, v243, s62\n"
"v_perm_b32 v70, v247, v243, s63\n"
"s_waitcnt vmcnt(45)\n"
"v_perm_b32 v43, v252, v248, s62\n"
"v_perm_b32 v47, v252, v248, s63\n"
"v_perm_b32 v51, v253, v249, s62\n"
"v_perm_b32 v55, v253, v249, s63\n"
"v_perm_b32 v59, v254, v250, s62\n"
"v_perm_b32 v63, v254, v250, s63\n"
"v_perm_b32 v67, v255, v251, s62\n"
"v_perm_b32 v71, v255, v251, s63\n"
"ds_write_b128 v32, v[40:43] offset:5120\n"
"ds_write_b128 v32, v[44:47] offset:7168\n"
"ds_write_b128 v33, v[48:51] offset:5120\n"
"ds_write_b128 v33, v[52:55] offset:7168\n"
"ds_write_b128 v34, v[56:59] offset:5120\n"
"ds_write_b128 v34, v[60:63] offset:7168\n"
"ds_write_b128 v35, v[64:67] offset:5120\n"
"ds_write_b128 v35, v[68:71] offset:7168\n"
"s_waitcnt lgkmcnt(0)\n"
"s_barrier\n"
"ds_read_b128 a[128:131], v2 offset:0\n"
"ds_read_b128 a[132:135], v2 offset:1024\n"
"ds_read_b128 a[136:139], v2 offset:2048\n"
"ds_read_b128 a[140:143], v2 offset:3072\n"
"ds_read_b128 a[144:147], v2 offset:4096\n"
"ds_read_b128 a[148:151], v2 offset:5120\n"
"ds_read_b128 a[152:155], v2 offset:6144\n"
"ds_read_b128 a[156:159], v2 offset:7168\n"
"ds_read_b128 a[160:163], v2 offset:8192\n"
"ds_read_b128 a[164:167], v2 offset:9216\n"
"ds_read_b128 a[168:171], v2 offset:10240\n"
"ds_read_b128 a[172:175], v2 offset:11264\n"
"ds_read_b128 a[176:179], v2 offset:12288\n"
"ds_read_b128 a[180:183], v2 offset:13312\n"
"ds_read_b128 a[184:187], v2 offset:14336\n"
"ds_read_b128 a[188:191], v2 offset:15360\n"
"ds_read_b128 a[192:195], v2 offset:16384\n"
"ds_read_b128 a[196:199], v2 offset:17408\n"
"ds_read_b128 a[200:203], v2 offset:18432\n"
"ds_read_b128 a[204:207], v2 offset:19456\n"
"ds_read_b128 a[208:211], v2 offset:20480\n"
"ds_read_b128 a[212:215], v2 offset:21504\n"
"ds_read_b128 a[216:219], v2 offset:22528\n"
"ds_read_b128 a[220:223], v2 offset:23552\n"
"ds_read_b128 a[224:227], v2 offset:24576\n"
"ds_read_b128 a[228:231], v2 offset:25600\n"
"ds_read_b128 a[232:235], v2 offset:26624\n"
"ds_read_b128 a[236:239], v2 offset:27648\n"
"ds_read_b128 a[240:243], v2 offset:28672\n"
"ds_read_b128 a[244:247], v2 offset:29696\n"
"ds_read_b128 a[248:251], v2 offset:30720\n"
"ds_read_b128 a[252:255], v2 offset:31744\n"
"s_waitcnt lgkmcnt(0)\n"
"s_barrier\n"
"s_cmp_lt_i32 %5, 0\n"
"s_cbranch_scc1 LH_CC2%=\n"
"s_cmp_lg_u32 s49, 0\n"
"s_cbranch_scc1 LH_CC1%=\n"
"s_waitcnt vmcnt(44)\n"
"v_readfirstlane_b32 s28, v20\n"
"v_readfirstlane_b32 s29, v21\n"
"s_cmp_eq_u64 s[28:29], s[66:67]\n"
"s_cselect_b32 s28, 1, 0\n"
"s_mov_b32 s29, 0\n"
"s_and_b32 s73, s68, -2\n"
"s_or_b32 s73, s73, s28\n"
"v_mov_b32 v18, s73\n"
"v_mov_b32 v19, s69\n"
"v_mov_b32 v20, s28\n"
"s_lshl_b32 s73, %6, 3\n"
"s_add_u32 s73, s73, 40960\n"
"s_mov_b64 exec, 1\n"
"buffer_store_dwordx2 v[18:19], v22, s[40:43], s73 offen sc1\n"
"ds_write_b32 v22, v20\n"
"s_mov_b64 exec, -1\n"
"LH_CC1%=:\n"
"s_waitcnt lgkmcnt(0)\n"
"s_barrier\n"
"ds_read_b32 v20, v22\n"
"s_waitcnt lgkmcnt(0)\n"
"v_readfirstlane_b32 %0, v20\n"
"s_barrier\n"
"s_cmp_lg_u32 %0, 0\n"
"s_cbranch_scc1 LH_END%=\n"
"LH_CC2%=:\n"
"v_mov_b32 v38, 0\n"
"s_mov_b32 s26, 38656\n"
"buffer_load_dwordx2 v[36:37], v38, s[40:43], s26 offen sc1\n"
"s_mov_b32 s73, 18432\n"
"buffer_load_dwordx4 v[176:179], v30, s[44:47], s73 offen\n"
"s_mov_b32 s73, 26624\n"
"buffer_load_dwordx4 v[180:183], v30, s[44:47], s73 offen\n"
"s_mov_b32 s73, 51200\n"
"buffer_load_dwordx4 v[184:187], v30, s[44:47], s73 offen\n"
"s_mov_b32 s73, 59392\n"
"buffer_load_dwordx4 v[188:191], v30, s[44:47], s73 offen\n"
"s_mov_b32 s73, 4096\n"
"buffer_load_dwordx4 v[192:195], v30, s[44:47], s73 offen\n"
"s_mov_b32 s73, 12288\n"
"buffer_load_dwordx4 v[196:199], v30, s[44:47], s73 offen\n"
"s_mov_b32 s73, 36864\n"
"buffer_load_dwordx4 v[200:203], v30, s[44:47], s73 offen\n"
"s_mov_b32 s73, 45056\n"
"buffer_load_dwordx4 v[204:207], v30, s[44:47], s73 offen\n"
"s_mov_b32 s73, 20480\n"
"buffer_load_dwordx4 v[208:211], v30, s[44:47], s73 offen\n"
"s_mov_b32 s73, 28672\n"
"buffer_load_dwordx4 v[212:215], v30, s[44:47], s73 offen\n"
"s_mov_b32 s73, 53248\n"
"buffer_load_dwordx4 v[216:219], v30, s[44:47], s73 offen\n"
"s_mov_b32 s73, 61440\n"
"buffer_load_dwordx4 v[220:223], v30, s[44:47], s73 offen\n"
"s_mov_b32 s73, 6144\n"
"buffer_load_dwordx4 v[224:227], v30, s[44:47], s73 offen\n"
"s_mov_b32 s73, 14336\n"
"buffer_load_dwordx4 v[228:231], v30, s[44:47], s73 offen\n"
"s_mov_b32 s73, 38912\n"
"buffer_load_dwordx4 v[232:235], v30, s[44:47], s73 offen\n"
"s_mov_b32 s73, 47104\n"
"buffer_load_dwordx4 v[236:239], v30, s[44:47], s73 offen\n"
"s_waitcnt vmcnt(60)\n"
"v_accvgpr_read_b32 v72, a0\n"
"v_accvgpr_read_b32 v73, a1\n"
"v_accvgpr_read_b32 v74, a2\n"
"v_accvgpr_read_b32 v75, a3\n"
"s_waitcnt vmcnt(59)\n"
"v_accvgpr_read_b32 v76, a4\n"
"v_accvgpr_read_b32 v77, a5\n"
"v_accvgpr_read_b32 v78, a6\n"
"v_accvgpr_read_b32 v79, a7\n"
"v_perm_b32 v40, v76, v72, s62\n"
"v_perm_b32 v44, v76, v72, s63\n"
"v_perm_b32 v48, v77, v73, s62\n"
"v_perm_b32 v52, v77, v73, s63\n"
"v_perm_b32 v56, v78, v74, s62\n"
"v_perm_b32 v60, v78, v74, s63\n"
"v_perm_b32 v64, v79, v75, s62\n"
"v_perm_b32 v68, v79, v75, s63\n"
"s_waitcnt vmcnt(58)\n"
"v_accvgpr_read_b32 v72, a8\n"
"v_accvgpr_read_b32 v73, a9\n"
"v_accvgpr_read_b32 v74, a10\n"
"v_accvgpr_read_b32 v75, a11\n"
"s_waitcnt vmcnt(57)\n"
"v_accvgpr_read_b32 v76, a12\n"
"v_accvgpr_read_b32 v77, a13\n"
"v_accvgpr_read_b32 v78, a14\n"
"v_accvgpr_read_b32 v79, a15\n"
"v_perm_b32 v41, v76, v72, s62\n"
"v_perm_b32 v45, v76, v72, s63\n"
"v_perm_b32 v49, v77, v73, s62\n"
"v_perm_b32 v53, v77, v73, s63\n"
"v_perm_b32 v57, v78, v74, s62\n"
"v_perm_b32 v61, v78, v74, s63\n"
"v_perm_b32 v65, v79, v75, s62\n"
"v_perm_b32 v69, v79, v75, s63\n"
"s_waitcnt vmcnt(56)\n"
"v_accvgpr_read_b32 v72, a16\n"
"v_accvgpr_read_b32 v73, a17\n"
"v_accvgpr_read_b32 v74, a18\n"
"v_accvgpr_read_b32 v75, a19\n"
"s_waitcnt vmcnt(55)\n"
"v_accvgpr_read_b32 v76, a20\n"
"v_accvgpr_read_b32 v77, a21\n"
"v_accvgpr_read_b32 v78, a22\n"
"v_accvgpr_read_b32 v79, a23\n"
"v_perm_b32 v42, v76, v72, s62\n"
"v_perm_b32 v46, v76, v72, s63\n"
"v_perm_b32 v50, v77, v73, s62\n"
"v_perm_b32 v54, v77, v73, s63\n"
"v_perm_b32 v58, v78, v74, s62\n"
"v_perm_b32 v62, v78, v74, s63\n"
"v_perm_b32 v66, v79, v75, s62\n"
"v_perm_b32 v70, v79, v75, s63\n"
"s_waitcnt vmcnt(54)\n"
"v_accvgpr_read_b32 v72, a24\n"
"v_accvgpr_read_b32 v73, a25\n"
"v_accvgpr_read_b32 v74, a26\n"
"v_accvgpr_read_b32 v75, a27\n"
"s_waitcnt vmcnt(53)\n"
"v_accvgpr_read_b32 v76, a28\n"
"v_accvgpr_read_b32 v77, a29\n"
"v_accvgpr_read_b32 v78, a30\n"
"v_accvgpr_read_b32 v79, a31\n"
"v_perm_b32 v43, v76, v72, s62\n"
"v_perm_b32 v47, v76, v72, s63\n"
"v_perm_b32 v51, v77, v73, s62\n"
"v_perm_b32 v55, v77, v73, s63\n"
"v_perm_b32 v59, v78, v74, s62\n"
"v_perm_b32 v63, v78, v74, s63\n"
"v_perm_b32 v67, v79, v75, s62\n"
"v_perm_b32 v71, v79, v75, s63\n"
"ds_write_b128 v32, v[40:43] offset:0\n"
"ds_write_b128 v32, v[44:47] offset:2048\n"
"ds_write_b128 v33, v[48:51] offset:0\n"
"ds_write_b128 v33, v[52:55] offset:2048\n"
"ds_write_b128 v34, v[56:59] offset:0\n"
"ds_write_b128 v34, v[60:63] offset:2048\n"
"ds_write_b128 v35, v[64:67] offset:0\n"
"ds_write_b128 v35, v[68:71] offset:2048\n"
"s_mov_b32 s73, 22528\n"
"buffer_load_dwordx4 v[240:243], v30, s[44:47], s73 offen\n"
"s_mov_b32 s73, 30720\n"
"buffer_load_dwordx4 v[244:247], v30, s[44:47], s73 offen\n"
"s_mov_b32 s73, 55296\n"
"buffer_load_dwordx4 v[248:251], v30, s[44:47], s73 offen\n"
"s_mov_b32 s73, 63488\n"
"buffer_load_dwordx4 v[252:255], v30, s[44:47], s73 offen\n"
"s_waitcnt vmcnt(56)\n"
"v_accvgpr_read_b32 v72, a32\n"
"v_accvgpr_read_b32 v73, a33\n"
"v_accvgpr_read_b32 v74, a34\n"
"v_accvgpr_read_b32 v75, a35\n"
"s_waitcnt vmcnt(55)\n"
"v_accvgpr_read_b32 v76, a36\n"
"v_accvgpr_read_b32 v77, a37\n"
"v_accvgpr_read_b32 v78, a38\n"
"v_accvgpr_read_b32 v79, a39\n"
"v_perm_b32 v40, v76, v72, s62\n"
"v_perm_b32 v44, v76, v72, s63\n"
"v_perm_b32 v48, v77, v73, s62\n"
"v_perm_b32 v52, v77, v73, s63\n"
"v_perm_b32 v56, v78, v74, s62\n"
"v_perm_b32 v60, v78, v74, s63\n"
"v_perm_b32 v64, v79, v75, s62\n"
"v_perm_b32 v68, v79, v75, s63\n"
"s_waitcnt vmcnt(54)\n"
"v_accvgpr_read_b32 v72, a40\n"
"v_accvgpr_read_b32 v73, a41\n"
"v_accvgpr_read_b32 v74, a42\n"
"v_accvgpr_read_b32 v75, a43\n"
"s_waitcnt vmcnt(53)\n"
"v_accvgpr_read_b32 v76, a44\n"
"v_accvgpr_read_b32 v77, a45\n"
"v_accvgpr_read_b32 v78, a46\n"
"v_accvgpr_read_b32 v79, a47\n"
"v_perm_b32 v41, v76, v72, s62\n"
"v_perm_b32 v45, v76, v72, s63\n"
"v_perm_b32 v49, v77, v73, s62\n"
"v_perm_b32 v53, v77, v73, s63\n"
"v_perm_b32 v57, v78, v74, s62\n"
"v_perm_b32 v61, v78, v74, s63\n"
"v_perm_b32 v65, v79, v75, s62\n"
"v_perm_b32 v69, v79, v75, s63\n"
"s_waitcnt vmcnt(52)\n"
"v_accvgpr_read_b32 v72, a48\n"
"v_accvgpr_read_b32 v73, a49\n"
"v_accvgpr_read_b32 v74, a50\n"
"v_accvgpr_read_b32 v75, a51\n"
"s_waitcnt vmcnt(51)\n"
"v_accvgpr_read_b32 v76, a52\n"
"v_accvgpr_read_b32 v77, a53\n"
"v_accvgpr_read_b32 v78, a54\n"
"v_accvgpr_read_b32 v79, a55\n"
"v_perm_b32 v42, v76, v72, s62\n"
"v_perm_b32 v46, v76, v72, s63\n"
"v_perm_b32 v50, v77, v73, s62\n"
"v_perm_b32 v54, v77, v73, s63\n"
"v_perm_b32 v58, v78, v74, s62\n"
"v_perm_b32 v62, v78, v74, s63\n"
"v_perm_b32 v66, v79, v75, s62\n"
"v_perm_b32 v70, v79, v75, s63\n"
"s_waitcnt vmcnt(50)\n"
"v_accvgpr_read_b32 v72, a56\n"
"v_accvgpr_read_b32 v73, a57\n"
"v_accvgpr_read_b32 v74, a58\n"
"v_accvgpr_read_b32 v75, a59\n"
"s_waitcnt vmcnt(49)\n"
"v_accvgpr_read_b32 v76, a60\n"
"v_accvgpr_read_b32 v77, a61\n"
"v_accvgpr_read_b32 v78, a62\n"
"v_accvgpr_read_b32 v79, a63\n"
"v_perm_b32 v43, v76, v72, s62\n"
"v_perm_b32 v47, v76, v72, s63\n"
"v_perm_b32 v51, v77, v73, s62\n"
"v_perm_b32 v55, v77, v73, s63\n"
"v_perm_b32 v59, v78, v74, s62\n"
"v_perm_b32 v63, v78, v74, s63\n"
"v_perm_b32 v67, v79, v75, s62\n"
"v_perm_b32 v71, v79, v75, s63\n"
"ds_write_b128 v32, v[40:43] offset:1024\n"
"ds_write_b128 v32, v[44:47] offset:3072\n"
"ds_write_b128 v33, v[48:51] offset:1024\n"
"ds_write_b128 v33, v[52:55] offset:3072\n"
"ds_write_b128 v34, v[56:59] offset:1024\n"
"ds_write_b128 v34, v[60:63] offset:3072\n"
"ds_write_b128 v35, v[64:67] offset:1024\n"
"ds_write_b128 v35, v[68:71] offset:3072\n"
"s_waitcnt vmcnt(48)\n"
"v_accvgpr_read_b32 v72, a64\n"
"v_accvgpr_read_b32 v73, a65\n"
"v_accvgpr_read_b32 v74, a66\n"
"v_accvgpr_read_b32 v75, a67\n"
"s_waitcnt vmcnt(47)\n"
"v_accvgpr_read_b32 v76, a68\n"
"v_accvgpr_read_b32 v77, a69\n"
"v_accvgpr_read_b32 v78, a70\n"
"v_accvgpr_read_b32 v79, a71\n"
"v_perm_b32 v40, v76, v72, s62\n"
"v_perm_b32 v44, v76, v72, s63\n"
"v_perm_b32 v48, v77, v73, s62\n"
"v_perm_b32 v52, v77, v73, s63\n"
"v_perm_b32 v56, v78, v74, s62\n"
"v_perm_b32 v60, v78, v74, s63\n"
"v_perm_b32 v64, v79, v75, s62\n"
"v_perm_b32 v68, v79, v75, s63\n"
"s_waitcnt vmcnt(46)\n"
"v_accvgpr_read_b32 v72, a72\n"
"v_accvgpr_read_b32 v73, a73\n"
"v_accvgpr_read_b32 v74, a74\n"
"v_accvgpr_read_b32 v75, a75\n"
"s_waitcnt vmcnt(45)\n"
"v_accvgpr_read_b32 v76, a76\n"
"v_accvgpr_read_b32 v77, a77\n"
"v_accvgpr_read_b32 v78, a78\n"
"v_accvgpr_read_b32 v79, a79\n"
"v_perm_b32 v41, v76, v72, s62\n"
"v_perm_b32 v45, v76, v72, s63\n"
"v_perm_b32 v49, v77, v73, s62\n"
"v_perm_b32 v53, v77, v73, s63\n"
"v_perm_b32 v57, v78, v74, s62\n"
"v_perm_b32 v61, v78, v74, s63\n"
"v_perm_b32 v65, v79, v75, s62\n"
"v_perm_b32 v69, v79, v75, s63\n"
"s_waitcnt vmcnt(44)\n"
"v_accvgpr_read_b32 v72, a80\n"
"v_accvgpr_read_b32 v73, a81\n"
"v_accvgpr_read_b32 v74, a82\n"
"v_accvgpr_read_b32 v75, a83\n"
"s_waitcnt vmcnt(43)\n"
"v_accvgpr_read_b32 v76, a84\n"
"v_accvgpr_read_b32 v77, a85\n"
"v_accvgpr_read_b32 v78, a86\n"
"v_accvgpr_read_b32 v79, a87\n"
"v_perm_b32 v42, v76, v72, s62\n"
"v_perm_b32 v46, v76, v72, s63\n"
"v_perm_b32 v50, v77, v73, s62\n"
"v_perm_b32 v54, v77, v73, s63\n"
"v_perm_b32 v58, v78, v74, s62\n"
"v_perm_b32 v62, v78, v74, s63\n"
"v_perm_b32 v66, v79, v75, s62\n"
"v_perm_b32 v70, v79, v75, s63\n"
"s_waitcnt vmcnt(42)\n"
"v_accvgpr_read_b32 v72, a88\n"
"v_accvgpr_read_b32 v73, a89\n"
"v_accvgpr_read_b32 v74, a90\n"
"v_accvgpr_read_b32 v75, a91\n"
"s_waitcnt vmcnt(41)\n"
"v_accvgpr_read_b32 v76, a92\n"
"v_accvgpr_read_b32 v77, a93\n"
"v_accvgpr_read_b32 v78, a94\n"
"v_accvgpr_read_b32 v79, a95\n"
"v_perm_b32 v43, v76, v72, s62\n"
"v_perm_b32 v47, v76, v72, s63\n"
"v_perm_b32 v51, v77, v73, s62\n"
"v_perm_b32 v55, v77, v73, s63\n"
"v_perm_b32 v59, v78, v74, s62\n"
"v_perm_b32 v63, v78, v74, s63\n"
"v_perm_b32 v67, v79, v75, s62\n"
"v_perm_b32 v71, v79, v75, s63\n"
"ds_write_b128 v32, v[40:43] offset:4096\n"
"ds_write_b128 v32, v[44:47] offset:6144\n"
"ds_write_b128 v33, v[48:51] offset:4096\n"
"ds_write_b128 v33, v[52:55] offset:6144\n"
"ds_write_b128 v34, v[56:59] offset:4096\n"
"ds_write_b128 v34, v[60:63] offset:6144\n"
"ds_write_b128 v35, v[64:67] offset:4096\n"
"ds_write_b128 v35, v[68:71] offset:6144\n"
"s_waitcnt vmcnt(40)\n"
"v_accvgpr_read_b32 v72, a96\n"
"v_accvgpr_read_b32 v73, a97\n"
"v_accvgpr_read_b32 v74, a98\n"
"v_accvgpr_read_b32 v75, a99\n"
"s_waitcnt vmcnt(39)\n"
"v_accvgpr_read_b32 v76, a100\n"
"v_accvgpr_read_b32 v77, a101\n"
"v_accvgpr_read_b32 v78, a102\n"
"v_accvgpr_read_b32 v79, a103\n"
"v_perm_b32 v40, v76, v72, s62\n"
"v_perm_b32 v44, v76, v72, s63\n"
"v_perm_b32 v48, v77, v73, s62\n"
"v_perm_b32 v52, v77, v73, s63\n"
"v_perm_b32 v56, v78, v74, s62\n"
"v_perm_b32 v60, v78, v74, s63\n"
"v_perm_b32 v64, v79, v75, s62\n"
"v_perm_b32 v68, v79, v75, s63\n"
"s_waitcnt vmcnt(38)\n"
"v_accvgpr_read_b32 v72, a104\n"
"v_accvgpr_read_b32 v73, a105\n"
"v_accvgpr_read_b32 v74, a106\n"
"v_accvgpr_read_b32 v75, a107\n"
"s_waitcnt vmcnt(37)\n"
"v_accvgpr_read_b32 v76, a108\n"
"v_accvgpr_read_b32 v77, a109\n"
"v_accvgpr_read_b32 v78, a110\n"
"v_accvgpr_read_b32 v79, a111\n"
"v_perm_b32 v41, v76, v72, s62\n"
"v_perm_b32 v45, v76, v72, s63\n"
"v_perm_b32 v49, v77, v73, s62\n"
"v_perm_b32 v53, v77, v73, s63\n"
"v_perm_b32 v57, v78, v74, s62\n"
"v_perm_b32 v61, v78, v74, s63\n"
"v_perm_b32 v65, v79, v75, s62\n"
"v_perm_b32 v69, v79, v75, s63\n"
"s_waitcnt vmcnt(36)\n"
"v_accvgpr_read_b32 v72, a112\n"
"v_accvgpr_read_b32 v73, a113\n"
"v_accvgpr_read_b32 v74, a114\n"
"v_accvgpr_read_b32 v75, a115\n"
"s_waitcnt vmcnt(35)\n"
"v_accvgpr_read_b32 v76, a116\n"
"v_accvgpr_read_b32 v77, a117\n"
"v_accvgpr_read_b32 v78, a118\n"
"v_accvgpr_read_b32 v79, a119\n"
"v_perm_b32 v42, v76, v72, s62\n"
"v_perm_b32 v46, v76, v72, s63\n"
"v_perm_b32 v50, v77, v73, s62\n"
"v_perm_b32 v54, v77, v73, s63\n"
"v_perm_b32 v58, v78, v74, s62\n"
"v_perm_b32 v62, v78, v74, s63\n"
"v_perm_b32 v66, v79, v75, s62\n"
"v_perm_b32 v70, v79, v75, s63\n"
"s_waitcnt vmcnt(34)\n"
"v_accvgpr_read_b32 v72, a120\n"
"v_accvgpr_read_b32 v73, a121\n"
"v_accvgpr_read_b32 v74, a122\n"
"v_accvgpr_read_b32 v75, a123\n"
"s_waitcnt vmcnt(33)\n"
"v_accvgpr_read_b32 v76, a124\n"
"v_accvgpr_read_b32 v77, a125\n"
"v_accvgpr_read_b32 v78, a126\n"
"v_accvgpr_read_b32 v79, a127\n"
"v_perm_b32 v43, v76, v72, s62\n"
"v_perm_b32 v47, v76, v72, s63\n"
"v_perm_b32 v51, v77, v73, s62\n"
"v_perm_b32 v55, v77, v73, s63\n"
"v_perm_b32 v59, v78, v74, s62\n"
"v_perm_b32 v63, v78, v74, s63\n"
"v_perm_b32 v67, v79, v75, s62\n"
"v_perm_b32 v71, v79, v75, s63\n"
"ds_write_b128 v32, v[40:43] offset:5120\n"
"ds_write_b128 v32, v[44:47] offset:7168\n"
"ds_write_b128 v33, v[48:51] offset:5120\n"
"ds_write_b128 v33, v[52:55] offset:7168\n"
"ds_write_b128 v34, v[56:59] offset:5120\n"
"ds_write_b128 v34, v[60:63] offset:7168\n"
"ds_write_b128 v35, v[64:67] offset:5120\n"
"ds_write_b128 v35, v[68:71] offset:7168\n"
"s_waitcnt lgkmcnt(0)\n"
"s_barrier\n"
"ds_read_b128 a[0:3], v2 offset:0\n"
"ds_read_b128 a[4:7], v2 offset:1024\n"
"ds_read_b128 a[8:11], v2 offset:2048\n"
"ds_read_b128 a[12:15], v2 offset:3072\n"
"ds_read_b128 a[16:19], v2 offset:4096\n"
"ds_read_b128 a[20:23], v2 offset:5120\n"
"ds_read_b128 a[24:27], v2 offset:6144\n"
"ds_read_b128 a[28:31], v2 offset:7168\n"
"ds_read_b128 a[32:35], v2 offset:8192\n"
"ds_read_b128 a[36:39], v2 offset:9216\n"
"ds_read_b128 a[40:43], v2 offset:10240\n"
"ds_read_b128 a[44:47], v2 offset:11264\n"
"ds_read_b128 a[48:51], v2 offset:12288\n"
"ds_read_b128 a[52:55], v2 offset:13312\n"
"ds_read_b128 a[56:59], v2 offset:14336\n"
"ds_read_b128 a[60:63], v2 offset:15360\n"
"ds_read_b128 a[64:67], v2 offset:16384\n"
"ds_read_b128 a[68:71], v2 offset:17408\n"
"ds_read_b128 a[72:75], v2 offset:18432\n"
"ds_read_b128 a[76:79], v2 offset:19456\n"
"ds_read_b128 a[80:83], v2 offset:20480\n"
"ds_read_b128 a[84:87], v2 offset:21504\n"
"ds_read_b128 a[88:91], v2 offset:22528\n"
"ds_read_b128 a[92:95], v2 offset:23552\n"
"ds_read_b128 a[96:99], v2 offset:24576\n"
"ds_read_b128 a[100:103], v2 offset:25600\n"
"ds_read_b128 a[104:107], v2 offset:26624\n"
"ds_read_b128 a[108:111], v2 offset:27648\n"
"ds_read_b128 a[112:115], v2 offset:28672\n"
"ds_read_b128 a[116:119], v2 offset:29696\n"
"ds_read_b128 a[120:123], v2 offset:30720\n"
"ds_read_b128 a[124:127], v2 offset:31744\n"
"s_waitcnt lgkmcnt(0)\n"
"s_barrier\n"
"s_waitcnt vmcnt(20)\n"
"v_readfirstlane_b32 s73, v36\n"
"v_readfirstlane_b32 s67, v37\n"
"s_or_b32 s66, s73, 1\n"
"s_cmp_eq_u64 s[66:67], s[68:69]\n"
"s_cbranch_scc1 LH_SELOK%=\n"
"v_mov_b32 v34, 0\n"
"s_mov_b32 s72, 0\n"
"s_mov_b32 s26, 38656\n"
"LH_SEL%=:\n"
"buffer_load_dwordx2 v[36:37], v34, s[40:43], s26 offen sc1\n"
"s_waitcnt vmcnt(0) lgkmcnt(0)\n"
"v_readfirstlane_b32 s73, v36\n"
"v_readfirstlane_b32 s74, v37\n"
"s_add_u32 s72, s72, 1\n"
"s_cmp_gt_u32 s72, 400000\n"
"s_cbranch_scc1 LH_FAIL%=\n"
"s_mov_b32 s66, s73\n"
"s_mov_b32 s67, s74\n"
"s_or_b32 s66, s66, 1\n"
"s_cmp_lg_u64 s[66:67], s[68:69]\n"
"s_cbranch_scc1 LH_SEL%=\n"
"s_mov_b32 s72, 0\n"
"LH_SELOK%=:\n"
"s_and_b32 s75, s73, 1\n"
"s_lshl_b32 s75, s75, 13\n"
"s_add_u32 s75, s75, 2097152\n"
"v_and_b32 v32, 7, v24\n"
"v_mul_u32_u24 v32, 544, v32\n"
"v_lshlrev_b32 v33, 4, v25\n"
"v_add_u32 v32, v32, v33\n"
"v_add_u32 v7, 40960, v32\n"
"v_lshrrev_b32 v32, 2, v24\n"
"v_and_b32 v34, 2, v24\n"
"v_xor_b32 v32, v32, v34\n"
"v_lshlrev_b32 v32, 1, v32\n"
"v_and_b32 v34, 1, v24\n"
"v_add_u32 v32, v32, v34\n"
"v_mul_u32_u24 v32, 544, v32\n"
"v_add_u32 v32, v32, v33\n"
"v_add_u32 v8, 41216, v32\n"
"v_mul_u32_u24 v32, 1088, v25\n"
"v_lshlrev_b32 v34, 2, v24\n"
"v_add_u32 v32, v32, v34\n"
"s_lshl_b32 s73, s49, 6\n"
"s_add_u32 s73, s73, 40960\n"
"v_add_u32 v9, s73, v32\n"
"v_lshrrev_b32 v32, 1, v24\n"
"v_lshlrev_b32 v32, 2, v32\n"
"v_lshlrev_b32 v34, 1, v27\n"
"v_add_u32 v32, v32, v34\n"
"v_and_b32 v34, 1, v24\n"
"v_add_u32 v32, v32, v34\n"
"v_lshlrev_b32 v32, 4, v32\n"
"v_lshlrev_b32 v34, 8, v26\n"
"v_add_u32 v32, v32, v34\n"
"s_lshl_b32 s73, s49, 9\n"
"s_add_u32 s73, s73, 45312\n"
"v_add_u32 v32, s73, v32\n"
"v_and_b32 v34, 7, v24\n"
"v_lshlrev_b32 v35, 3, v25\n"
"v_add_u32 v34, v34, v35\n"
"v_lshlrev_b32 v34, 4, v34\n"
"v_add_u32 v34, 47360, v34\n"
"v_cmp_gt_u32 vcc, 8, v24\n"
"s_nop 1\n"
"v_cndmask_b32 v10, v34, v32, vcc\n"
"v_lshrrev_b32 v32, 2, v24\n"
"v_lshlrev_b32 v32, 14, v32\n"
"v_lshlrev_b32 v34, 11, v25\n"
"v_add_u32 v32, v32, v34\n"
"v_and_b32 v35, 3, v15\n"
"v_lshlrev_b32 v34, 4, v35\n"
"v_add_u32 v32, v32, v34\n"
"s_lshl_b32 s73, s49, 17\n"
"s_lshl_b32 s74, s48, 6\n"
"s_add_u32 s73, s73, s74\n"
"s_add_u32 s73, s73, s75\n"
"v_add_u32 v11, s73, v32\n"
"v_mov_b32 v16, 0\n"
"v_mov_b32 v17, 0\n"
"v_mov_b32 v18, 0\n"
"v_mov_b32 v19, 0\n"
"v_mov_b32 v21, 0\n"
"v_mov_b32 v22, 0\n"
"v_mov_b32 v23, 0\n"
"s_mov_b32 s52, 0x80000\n"
"s_mov_b32 s53, 0x90000\n"
"s_lshl_b32 s74, s48, 13\n"
"s_add_u32 s74, s74, s87\n"
"s_add_u32 s57, s74, 4194304\n"
"s_add_u32 s58, s74, 20971520\n"
"s_add_u32 s66, s87, 8192\n"
"v_add_u32 v1, 8192, v0\n"
"v_add_u32 v2, s87, v1\n"
"v_add_u32 v3, s87, v0\n"
"s_lshl_b32 s73, s49, 1\n"
"s_add_u32 s73, s73, 2\n"
"s_and_b32 s73, s73, 7\n"
"s_lshl_b32 s73, s73, 10\n"
"v_add_u32 v4, s73, v0\n"
"s_lshl_b32 s73, s49, 1\n"
"s_add_u32 s73, s73, 4\n"
"s_and_b32 s73, s73, 7\n"
"s_lshl_b32 s73, s73, 10\n"
"v_add_u32 v5, s73, v0\n"
"s_lshl_b32 s73, s49, 1\n"
"s_add_u32 s73, s73, 6\n"
"s_and_b32 s73, s73, 7\n"
"s_lshl_b32 s73, s73, 10\n"
"v_add_u32 v6, s73, v0\n"
"v_lshlrev_b32 v13, 2, v15\n"
"v_bfe_u32 v32, v15, 1, 1\n"
"v_mul_u32_u24 v32, 0xaaaa, v32\n"
"v_add_u32 v14, 0x4444, v32\n"
"s_add_u32 s69, s75, 0x100000\n"
"s_mov_b32 s55, s69\n"
"s_add_u32 s56, s55, s87\n"
"s_mov_b32 s61, s56\n"
"s_lshr_b32 s73, s48, 2\n"
"s_lshl_b32 s73, s73, 14\n"
"s_and_b32 s74, s48, 3\n"
"s_lshl_b32 s74, s74, 11\n"
"s_add_u32 s73, s73, s74\n"
"s_lshl_b32 s74, s49, 9\n"
"s_add_u32 s73, s73, s74\n"
"s_add_u32 s73, s73, s75\n"
"s_add_u32 s68, s73, 0x140000\n"
"s_mov_b32 s67, s68\n"
"s_lshl_b32 s73, s48, 11\n"
"s_lshl_b32 s74, s49, 9\n"
"s_add_u32 s73, s73, s74\n"
"s_add_u32 s59, s73, 37748736\n"
"s_add_u32 s60, s73, 41943040\n"
"s_add_u32 s93, s75, 0x1c0000\n"
"s_mov_b32 s92, s93\n"
"s_add_u32 s79, s93, 896\n"
"v_lshrrev_b32 v32, 2, v24\n"
"v_lshlrev_b32 v32, 1, v32\n"
"v_and_b32 v34, 1, v24\n"
"v_add_u32 v32, v32, v34\n"
"v_lshlrev_b32 v32, 10, v32\n"
"v_lshlrev_b32 v34, 6, v25\n"
"v_add_u32 v32, v32, v34\n"
"v_bfe_u32 v34, v24, 1, 1\n"
"v_lshlrev_b32 v34, 5, v34\n"
"v_add_u32 v32, v32, v34\n"
"s_mov_b64 s[44:45], s[30:31]\n"
"s_mov_b32 s46, 8192\n"
"s_lshl_b32 s73, s49, 8\n"
"buffer_load_dwordx2 v[24:25], v32, s[44:47], s73 offen\n"
"buffer_load_dwordx2 v[26:27], v32, s[44:47], s73 offen offset:16\n"
"buffer_load_dwordx2 v[28:29], v32, s[44:47], s73 offen offset:8\n"
"buffer_load_dwordx2 v[30:31], v32, s[44:47], s73 offen offset:24\n"
"s_lshl_b32 s73, s48, 7\n"
"s_add_u32 s99, s73, 1024\n"
"v_mov_b32 v34, 0\n"
"v_mov_b32 v35, 1\n"
"s_mov_b32 s73, 0\n"
"s_cmp_lg_u32 s49, 0\n"
"s_cbranch_scc1 LH_S1%=\n"
"s_mov_b64 exec, 1\n"
"buffer_atomic_add v35, v34, s[40:43], s73 offen sc1\n"
"s_mov_b64 exec, -1\n"
"LH_S1%=:\n"
"v_mov_b32 v34, 0\n"
"s_mov_b32 s72, 0\n"
"LH_KV0%=:\n"
"buffer_load_dwordx2 v[36:37], v34, s[40:43], s99 offen sc1\n"
"s_waitcnt vmcnt(0) lgkmcnt(0)\n"
"v_readfirstlane_b32 s73, v36\n"
"v_readfirstlane_b32 s74, v37\n"
"s_add_u32 s72, s72, 1\n"
"s_cmp_gt_u32 s72, 400000\n"
"s_cbranch_scc1 LH_FAIL%=\n"
"s_and_b32 s27, s73, 3\n"
"s_cmp_lg_u32 s27, 3\n"
"s_cbranch_scc1 LH_KV0%=\n"
"s_mov_b32 s72, 0\n"
"s_mov_b32 s80, s73\n"
"s_mov_b32 s81, s74\n"
"s_mov_b32 m0, s66\n"
"s_add_u32 s73, s66, 8192\n"
"buffer_load_dwordx4 v0, s[40:43], s57 offen sc1 lds\n"
"buffer_load_dwordx4 v0, s[40:43], s57 offen offset:1024 sc1 lds\n"
"s_mov_b32 m0, s73\n"
"s_xor_b32 s66, s66, 0x4000\n"
"buffer_load_dwordx4 v0, s[40:43], s58 offen sc1 lds\n"
"buffer_load_dwordx4 v0, s[40:43], s58 offen offset:1024 sc1 lds\n"
"s_add_u32 s57, s57, 0x40000\n"
"s_add_u32 s58, s58, 0x40000\n"
"s_mov_b32 m0, s66\n"
"s_add_u32 s73, s66, 8192\n"
"buffer_load_dwordx4 v0, s[40:43], s57 offen sc1 lds\n"
"buffer_load_dwordx4 v0, s[40:43], s57 offen offset:1024 sc1 lds\n"
"s_mov_b32 m0, s73\n"
"s_xor_b32 s66, s66, 0x4000\n"
"buffer_load_dwordx4 v0, s[40:43], s58 offen sc1 lds\n"
"buffer_load_dwordx4 v0, s[40:43], s58 offen offset:1024 sc1 lds\n"
"s_add_u32 s57, s57, 0x40000\n"
"s_add_u32 s58, s58, 0x40000\n"
"v_mov_b64 v[40:41], v[176:177]\n"
"v_mov_b64 v[42:43], v[178:179]\n"
"v_mov_b64 v[44:45], v[180:181]\n"
"v_mov_b64 v[46:47], v[182:183]\n"
"v_mov_b64 v[48:49], v[184:185]\n"
"v_mov_b64 v[50:51], v[186:187]\n"
"v_mov_b64 v[52:53], v[188:189]\n"
"v_mov_b64 v[54:55], v[190:191]\n"
"v_perm_b32 v128, v84, v80, s62\n"
"v_perm_b32 v136, v84, v80, s63\n"
"v_perm_b32 v144, v85, v81, s62\n"
"v_perm_b32 v152, v85, v81, s63\n"
"v_perm_b32 v160, v86, v82, s62\n"
"v_perm_b32 v168, v86, v82, s63\n"
"v_perm_b32 v176, v87, v83, s62\n"
"v_perm_b32 v184, v87, v83, s63\n"
"v_perm_b32 v129, v92, v88, s62\n"
"v_perm_b32 v137, v92, v88, s63\n"
"v_perm_b32 v145, v93, v89, s62\n"
"v_perm_b32 v153, v93, v89, s63\n"
"v_perm_b32 v161, v94, v90, s62\n"
"v_perm_b32 v169, v94, v90, s63\n"
"v_perm_b32 v177, v95, v91, s62\n"
"v_perm_b32 v185, v95, v91, s63\n"
"v_perm_b32 v130, v100, v96, s62\n"
"v_perm_b32 v138, v100, v96, s63\n"
"v_perm_b32 v146, v101, v97, s62\n"
"v_perm_b32 v154, v101, v97, s63\n"
"v_perm_b32 v162, v102, v98, s62\n"
"v_perm_b32 v170, v102, v98, s63\n"
"v_perm_b32 v178, v103, v99, s62\n"
"v_perm_b32 v186, v103, v99, s63\n"
"v_perm_b32 v131, v108, v104, s62\n"
"v_perm_b32 v139, v108, v104, s63\n"
"v_perm_b32 v147, v109, v105, s62\n"
"v_perm_b32 v155, v109, v105, s63\n"
"v_perm_b32 v163, v110, v106, s62\n"
"v_perm_b32 v171, v110, v106, s63\n"
"v_perm_b32 v179, v111, v107, s62\n"
"v_perm_b32 v187, v111, v107, s63\n"
"v_perm_b32 v132, v116, v112, s62\n"
"v_perm_b32 v140, v116, v112, s63\n"
"v_perm_b32 v148, v117, v113, s62\n"
"v_perm_b32 v156, v117, v113, s63\n"
"v_perm_b32 v164, v118, v114, s62\n"
"v_perm_b32 v172, v118, v114, s63\n"
"v_perm_b32 v180, v119, v115, s62\n"
"v_perm_b32 v188, v119, v115, s63\n"
"v_perm_b32 v133, v124, v120, s62\n"
"v_perm_b32 v141, v124, v120, s63\n"
"v_perm_b32 v149, v125, v121, s62\n"
"v_perm_b32 v157, v125, v121, s63\n"
"v_perm_b32 v165, v126, v122, s62\n"
"v_perm_b32 v173, v126, v122, s63\n"
"v_perm_b32 v181, v127, v123, s62\n"
"v_perm_b32 v189, v127, v123, s63\n"
"v_perm_b32 v134, v44, v40, s62\n"
"v_perm_b32 v142, v44, v40, s63\n"
"v_perm_b32 v150, v45, v41, s62\n"
"v_perm_b32 v158, v45, v41, s63\n"
"v_perm_b32 v166, v46, v42, s62\n"
"v_perm_b32 v174, v46, v42, s63\n"
"v_perm_b32 v182, v47, v43, s62\n"
"v_perm_b32 v190, v47, v43, s63\n"
"v_perm_b32 v135, v52, v48, s62\n"
"v_perm_b32 v143, v52, v48, s63\n"
"v_perm_b32 v151, v53, v49, s62\n"
"v_perm_b32 v159, v53, v49, s63\n"
"v_perm_b32 v167, v54, v50, s62\n"
"v_perm_b32 v175, v54, v50, s63\n"
"v_perm_b32 v183, v55, v51, s62\n"
"v_perm_b32 v191, v55, v51, s63\n"
"v_mov_b64 v[40:41], v[192:193]\n"
"v_mov_b64 v[42:43], v[194:195]\n"
"v_mov_b64 v[44:45], v[196:197]\n"
"v_mov_b64 v[46:47], v[198:199]\n"
"v_mov_b64 v[48:49], v[200:201]\n"
"v_mov_b64 v[50:51], v[202:203]\n"
"v_mov_b64 v[52:53], v[204:205]\n"
"v_mov_b64 v[54:55], v[206:207]\n"
"v_mov_b64 v[56:57], v[208:209]\n"
"v_mov_b64 v[58:59], v[210:211]\n"
"v_mov_b64 v[60:61], v[212:213]\n"
"v_mov_b64 v[62:63], v[214:215]\n"
"v_mov_b64 v[64:65], v[216:217]\n"
"v_mov_b64 v[66:67], v[218:219]\n"
"v_mov_b64 v[68:69], v[220:221]\n"
"v_mov_b64 v[70:71], v[222:223]\n"
"v_mov_b64 v[72:73], v[224:225]\n"
"v_mov_b64 v[74:75], v[226:227]\n"
"v_mov_b64 v[76:77], v[228:229]\n"
"v_mov_b64 v[78:79], v[230:231]\n"
"v_mov_b64 v[80:81], v[232:233]\n"
"v_mov_b64 v[82:83], v[234:235]\n"
"v_mov_b64 v[84:85], v[236:237]\n"
"v_mov_b64 v[86:87], v[238:239]\n"
"v_mov_b64 v[88:89], v[240:241]\n"
"v_mov_b64 v[90:91], v[242:243]\n"
"v_mov_b64 v[92:93], v[244:245]\n"
"v_mov_b64 v[94:95], v[246:247]\n"
"v_mov_b64 v[96:97], v[248:249]\n"
"v_mov_b64 v[98:99], v[250:251]\n"
"v_mov_b64 v[100:101], v[252:253]\n"
"v_mov_b64 v[102:103], v[254:255]\n"
"v_perm_b32 v192, v44, v40, s62\n"
"v_perm_b32 v200, v44, v40, s63\n"
"v_perm_b32 v208, v45, v41, s62\n"
"v_perm_b32 v216, v45, v41, s63\n"
"v_perm_b32 v224, v46, v42, s62\n"
"v_perm_b32 v232, v46, v42, s63\n"
"v_perm_b32 v240, v47, v43, s62\n"
"v_perm_b32 v248, v47, v43, s63\n"
"v_perm_b32 v193, v52, v48, s62\n"
"v_perm_b32 v201, v52, v48, s63\n"
"v_perm_b32 v209, v53, v49, s62\n"
"v_perm_b32 v217, v53, v49, s63\n"
"v_perm_b32 v225, v54, v50, s62\n"
"v_perm_b32 v233, v54, v50, s63\n"
"v_perm_b32 v241, v55, v51, s62\n"
"v_perm_b32 v249, v55, v51, s63\n"
"v_perm_b32 v194, v60, v56, s62\n"
"v_perm_b32 v202, v60, v56, s63\n"
"v_perm_b32 v210, v61, v57, s62\n"
"v_perm_b32 v218, v61, v57, s63\n"
"v_perm_b32 v226, v62, v58, s62\n"
"v_perm_b32 v234, v62, v58, s63\n"
"v_perm_b32 v242, v63, v59, s62\n"
"v_perm_b32 v250, v63, v59, s63\n"
"v_perm_b32 v195, v68, v64, s62\n"
"v_perm_b32 v203, v68, v64, s63\n"
"v_perm_b32 v211, v69, v65, s62\n"
"v_perm_b32 v219, v69, v65, s63\n"
"v_perm_b32 v227, v70, v66, s62\n"
"v_perm_b32 v235, v70, v66, s63\n"
"v_perm_b32 v243, v71, v67, s62\n"
"v_perm_b32 v251, v71, v67, s63\n"
"v_perm_b32 v196, v76, v72, s62\n"
"v_perm_b32 v204, v76, v72, s63\n"
"v_perm_b32 v212, v77, v73, s62\n"
"v_perm_b32 v220, v77, v73, s63\n"
"v_perm_b32 v228, v78, v74, s62\n"
"v_perm_b32 v236, v78, v74, s63\n"
"v_perm_b32 v244, v79, v75, s62\n"
"v_perm_b32 v252, v79, v75, s63\n"
"v_perm_b32 v197, v84, v80, s62\n"
"v_perm_b32 v205, v84, v80, s63\n"
"v_perm_b32 v213, v85, v81, s62\n"
"v_perm_b32 v221, v85, v81, s63\n"
"v_perm_b32 v229, v86, v82, s62\n"
"v_perm_b32 v237, v86, v82, s63\n"
"v_perm_b32 v245, v87, v83, s62\n"
"v_perm_b32 v253, v87, v83, s63\n"
"v_perm_b32 v198, v92, v88, s62\n"
"v_perm_b32 v206, v92, v88, s63\n"
"v_perm_b32 v214, v93, v89, s62\n"
"v_perm_b32 v222, v93, v89, s63\n"
"v_perm_b32 v230, v94, v90, s62\n"
"v_perm_b32 v238, v94, v90, s63\n"
"v_perm_b32 v246, v95, v91, s62\n"
"v_perm_b32 v254, v95, v91, s63\n"
"v_perm_b32 v199, v100, v96, s62\n"
"v_perm_b32 v207, v100, v96, s63\n"
"v_perm_b32 v215, v101, v97, s62\n"
"v_perm_b32 v223, v101, v97, s63\n"
"v_perm_b32 v231, v102, v98, s62\n"
"v_perm_b32 v239, v102, v98, s63\n"
"v_perm_b32 v247, v103, v99, s62\n"
"v_perm_b32 v255, v103, v99, s63\n"
"s_waitcnt vmcnt(0) lgkmcnt(0)\n"
"s_barrier\n"
"ds_read_b128 v[56:59], v1 offset:0\n"
"ds_read_b128 v[60:63], v1 offset:4096\n"
"ds_read_b128 v[64:67], v1 offset:1024\n"
"ds_read_b128 v[68:71], v1 offset:5120\n"
"ds_read_b128 v[72:75], v1 offset:2048\n"
"ds_read_b128 v[76:79], v1 offset:6144\n"
"ds_read_b128 v[80:83], v1 offset:3072\n"
"ds_read_b128 v[84:87], v1 offset:7168\n"
"ds_read_b128 v[120:123], v2 offset:8192\n"
"ds_read_b128 v[124:127], v2 offset:9216\n"
"v_xor_b32 v1, 0x4000, v1\n"
"v_xor_b32 v2, 0x4000, v2\n"
"v_mov_b64 v[104:105], 0\n"
"v_mov_b64 v[106:107], 0\n"
"v_mov_b64 v[108:109], 0\n"
"v_mov_b64 v[110:111], 0\n"
"v_mov_b64 v[112:113], 0\n"
"v_mov_b64 v[114:115], 0\n"
"v_mov_b64 v[116:117], 0\n"
"v_mov_b64 v[118:119], 0\n"
"ds_write_b128 v3, v[24:27]\n"
"ds_write_b128 v3, v[28:31] offset:1024\n"
"s_waitcnt vmcnt(0) lgkmcnt(0)\n"
"s_barrier\n"
"v_mov_b32 v34, 0\n"
"s_mov_b32 s72, 0\n"
"s_mov_b32 s26, 512\n"
"LH_S4%=:\n"
"buffer_load_dwordx2 v[36:37], v34, s[40:43], s26 offen sc1\n"
"s_waitcnt vmcnt(0) lgkmcnt(0)\n"
"v_readfirstlane_b32 s73, v36\n"
"v_readfirstlane_b32 s74, v37\n"
"s_add_u32 s72, s72, 1\n"
"s_cmp_gt_u32 s72, 400000\n"
"s_cbranch_scc1 LH_FAIL%=\n"
"s_cmp_eq_u32 s74, 0\n"
"s_cbranch_scc1 LH_S4%=\n"
"s_mov_b32 s72, 0\n"
"s_mov_b32 s90, s73\n"
"s_sub_u32 s91, s90, s88\n"
"s_sub_u32 s91, s91, 30\n"
"LH_S5%=:\n"
"s_memrealtime s[96:97]\n"
"s_waitcnt vmcnt(0) lgkmcnt(0)\n"
"s_sub_u32 s73, s91, s96\n"
"s_add_u32 s73, s73, 19\n"
"s_cmp_lt_u32 s73, 1000\n"
"s_cbranch_scc1 LH_S5%=\n"
"s_mov_b32 s78, s96\n"
"s_mov_b32 s72, 0\n"
".p2align 6\n"
"LH_STEP%=:\n"
"s_barrier\n"
"ds_read_b128 v[24:27], v3 offset:0\n"
"ds_read_b128 v[28:31], v3 offset:1024\n"
"ds_read_b128 v[32:35], v4 offset:0\n"
"s_waitcnt lgkmcnt(2)\n"
"v_smfmac_f32_16x16x64_bf16 v[104:107], v[24:27], a[0:7], v14\n"
"ds_read_b128 v[36:39], v4 offset:1024\n"
"v_smfmac_f32_16x16x64_bf16 v[108:111], v[24:27], a[8:15], v14\n"
"ds_read_b128 v[40:43], v5 offset:0\n"
"s_waitcnt lgkmcnt(3)\n"
"v_smfmac_f32_16x16x64_bf16 v[104:107], v[28:31], a[16:23], v14\n"
"ds_read_b128 v[44:47], v5 offset:1024\n"
"v_smfmac_f32_16x16x64_bf16 v[108:111], v[28:31], a[24:31], v14\n"
"ds_read_b128 v[48:51], v6 offset:0\n"
"s_waitcnt lgkmcnt(4)\n"
"v_smfmac_f32_16x16x64_bf16 v[104:107], v[32:35], a[32:39], v14\n"
"ds_read_b128 v[52:55], v6 offset:1024\n"
"v_smfmac_f32_16x16x64_bf16 v[108:111], v[32:35], a[40:47], v14\n"
"s_waitcnt lgkmcnt(4)\n"
"v_smfmac_f32_16x16x64_bf16 v[104:107], v[36:39], a[48:55], v14\n"
"v_smfmac_f32_16x16x64_bf16 v[108:111], v[36:39], a[56:63], v14\n"
"s_waitcnt lgkmcnt(0)\n"
"v_smfmac_f32_16x16x64_bf16 v[104:107], v[40:43], a[64:71], v14\n"
"v_smfmac_f32_16x16x64_bf16 v[108:111], v[40:43], a[72:79], v14\n"
"v_smfmac_f32_16x16x64_bf16 v[104:107], v[44:47], a[80:87], v14\n"
"v_smfmac_f32_16x16x64_bf16 v[108:111], v[44:47], a[88:95], v14\n"
"v_smfmac_f32_16x16x64_bf16 v[104:107], v[48:51], a[96:103], v14\n"
"v_smfmac_f32_16x16x64_bf16 v[104:107], v[52:55], a[112:119], v14\n"
"v_smfmac_f32_16x16x64_bf16 v[108:111], v[48:51], a[104:111], v14\n"
"v_smfmac_f32_16x16x64_bf16 v[108:111], v[52:55], a[120:127], v14\n"
"s_nop 1\n"
"v_smfmac_f32_16x16x64_bf16 v[112:115], v[24:27], a[128:135], v14\n"
"s_nop 1\n"
"v_smfmac_f32_16x16x64_bf16 v[116:119], v[24:27], a[136:143], v14\n"
"v_add_f32 v104, v104, v106\n"
"v_smfmac_f32_16x16x64_bf16 v[112:115], v[28:31], a[144:151], v14\n"
"v_add_f32 v105, v105, v107\n"
"v_add_f32 v108, v108, v110\n"
"v_smfmac_f32_16x16x64_bf16 v[116:119], v[28:31], a[152:159], v14\n"
"v_add_f32 v109, v109, v111\n"
"v_cvt_pk_bf16_f32 v106, v104, v108\n"
"v_smfmac_f32_16x16x64_bf16 v[112:115], v[32:35], a[160:167], v14\n"
"v_cvt_pk_bf16_f32 v107, v105, v109\n"
"ds_write2_b32 v9, v106, v107 offset0:0 offset1:136\n"
"v_smfmac_f32_16x16x64_bf16 v[116:119], v[32:35], a[168:175], v14\n"
"v_cmp_u_f32 vcc, v104, v105\n"
"v_smfmac_f32_16x16x64_bf16 v[112:115], v[36:39], a[176:183], v14\n"
"v_smfmac_f32_16x16x64_bf16 v[116:119], v[36:39], a[184:191], v14\n"
"s_cbranch_vccnz LH_RETRY2%=\n"
"LH_CONT2%=:\n"
"v_smfmac_f32_16x16x64_bf16 v[112:115], v[40:43], a[192:199], v14\n"
"v_smfmac_f32_16x16x64_bf16 v[116:119], v[40:43], a[200:207], v14\n"
"s_waitcnt lgkmcnt(0)\n"
"s_barrier\n"
"ds_read_b128 v[88:91], v7 offset:0\n"
"v_smfmac_f32_16x16x64_bf16 v[112:115], v[44:47], a[208:215], v14\n"
"ds_read_b128 v[92:95], v7 offset:64\n"
"v_smfmac_f32_16x16x64_bf16 v[116:119], v[44:47], a[216:223], v14\n"
"ds_read_b128 v[96:99], v7 offset:128\n"
"v_smfmac_f32_16x16x64_bf16 v[112:115], v[48:51], a[224:231], v14\n"
"ds_read_b128 v[100:103], v7 offset:192\n"
"v_smfmac_f32_16x16x64_bf16 v[112:115], v[52:55], a[240:247], v14\n"
"v_smfmac_f32_16x16x64_bf16 v[116:119], v[48:51], a[232:239], v14\n"
"v_smfmac_f32_16x16x64_bf16 v[116:119], v[52:55], a[248:255], v14\n"
"s_waitcnt lgkmcnt(3)\n"
"v_mfma_f32_16x16x32_bf16 v[104:107], v[56:59], v[88:91], 0\n"
"s_nop 3\n"
"v_add_f32 v112, v112, v114\n"
"v_add_f32 v113, v113, v115\n"
"v_mfma_f32_16x16x32_bf16 v[108:111], v[60:63], v[88:91], 0\n"
"s_waitcnt lgkmcnt(0)\n"
"v_add_f32 v116, v116, v118\n"
"v_mfma_f32_16x16x32_bf16 v[104:107], v[64:67], v[92:95], v[104:107]\n"
"v_add_f32 v117, v117, v119\n"
"v_cvt_pk_bf16_f32 v114, v112, v116\n"
"v_mfma_f32_16x16x32_bf16 v[108:111], v[68:71], v[92:95], v[108:111]\n"
"v_cvt_pk_bf16_f32 v115, v113, v117\n"
"ds_write2_b32 v9, v114, v115 offset0:64 offset1:200\n"
"v_mfma_f32_16x16x32_bf16 v[104:107], v[72:75], v[96:99], v[104:107]\n"
"buffer_load_dword v16, v13, s[40:43], s67 offen sc1\n"
"buffer_load_dword v18, v13, s[40:43], s67 offen offset:256 sc1\n"
"v_mfma_f32_16x16x32_bf16 v[108:111], v[76:79], v[96:99], v[108:111]\n"
"v_mov_b64 v[56:57], 0\n"
"v_mov_b64 v[58:59], 0\n"
"v_mfma_f32_16x16x32_bf16 v[104:107], v[80:83], v[100:103], v[104:107]\n"
"v_mov_b64 v[60:61], 0\n"
"v_mov_b64 v[62:63], 0\n"
"v_mfma_f32_16x16x32_bf16 v[108:111], v[84:87], v[100:103], v[108:111]\n"
"v_mov_b64 v[64:65], 0\n"
"v_mov_b64 v[66:67], 0\n"
"s_cmp_eq_u32 s50, s51\n"
"s_waitcnt lgkmcnt(0)\n"
"s_barrier\n"
"s_cbranch_scc1 LH_DONE%=\n"
"ds_read_b128 v[24:27], v8 offset:0\n"
"ds_read_b128 v[28:31], v8 offset:64\n"
"ds_read_b128 v[32:35], v8 offset:128\n"
"ds_read_b128 v[36:39], v8 offset:192\n"
"v_max3_f32 v15, v104, v105, v106\n"
"v_max3_f32 v12, v107, v108, v109\n"
"v_max3_f32 v15, v15, v110, v111\n"
"v_max_f32 v15, v15, v12\n"
"v_mov_b32 v12, v15\n"
"v_mov_b64 v[68:69], 0\n"
"v_mov_b64 v[70:71], 0\n"
"v_permlane16_swap_b32 v15, v12\n"
"v_max_f32 v15, v15, v12\n"
"v_mov_b32 v12, v15\n"
"v_mov_b64 v[72:73], 0\n"
"s_nop 0\n"
"v_permlane32_swap_b32 v15, v12\n"
"v_max_f32 v15, v15, v12\n"
"v_mul_f32 v40, s64, v15\n"
"v_pk_fma_f32 v[104:105], v[104:105], s[64:65], v[40:41] op_sel_hi:[1,0,0] neg_lo:[0,0,1] neg_hi:[0,0,1]\n"
"v_pk_fma_f32 v[106:107], v[106:107], s[64:65], v[40:41] op_sel_hi:[1,0,0] neg_lo:[0,0,1] neg_hi:[0,0,1]\n"
"v_pk_fma_f32 v[108:109], v[108:109], s[64:65], v[40:41] op_sel_hi:[1,0,0] neg_lo:[0,0,1] neg_hi:[0,0,1]\n"
"v_pk_fma_f32 v[110:111], v[110:111], s[64:65], v[40:41] op_sel_hi:[1,0,0] neg_lo:[0,0,1] neg_hi:[0,0,1]\n"
"s_waitcnt lgkmcnt(0)\n"
"v_mfma_f32_16x16x32_bf16 v[112:115], v[24:27], v[88:91], 0\n"
"v_exp_f32 v104, v104\n"
"v_mfma_f32_16x16x32_bf16 v[112:115], v[28:31], v[92:95], v[112:115]\n"
"v_exp_f32 v105, v105\n"
"v_mfma_f32_16x16x32_bf16 v[112:115], v[32:35], v[96:99], v[112:115]\n"
"v_exp_f32 v106, v106\n"
"v_mfma_f32_16x16x32_bf16 v[112:115], v[36:39], v[100:103], v[112:115]\n"
"v_exp_f32 v107, v107\n"
"v_exp_f32 v108, v108\n"
"v_exp_f32 v109, v109\n"
"v_exp_f32 v110, v110\n"
"v_exp_f32 v111, v111\n"
"v_cvt_pk_bf16_f32 v24, v104, v105\n"
"v_cvt_pk_bf16_f32 v25, v106, v107\n"
"v_cvt_pk_bf16_f32 v26, v108, v109\n"
"v_max3_f32 v42, v15, v112, v113\n"
"v_cvt_pk_bf16_f32 v27, v110, v111\n"
"v_max3_f32 v42, v42, v114, v115\n"
"v_mov_b32 v43, v42\n"
"v_mfma_f32_16x16x32_bf16 v[48:51], v[120:123], v[24:27], 0\n"
"v_add_f32 v104, v104, v106\n"
"v_permlane16_swap_b32 v42, v43\n"
"v_mfma_f32_16x16x32_bf16 v[52:55], v[124:127], v[24:27], 0\n"
"v_max_f32 v42, v42, v43\n"
"v_mul_f32 v42, s64, v42\n"
"v_sub_f32 v44, v40, v42\n"
"v_fma_f32 v112, v112, s64, -v42\n"
"v_fma_f32 v113, v113, s64, -v42\n"
"s_waitcnt vmcnt(0)\n"
"v_max_u32 v12, v16, v18\n"
"v_exp_f32 v44, v44\n"
"v_cmp_eq_u32 vcc, -1, v12\n"
"v_exp_f32 v112, v112\n"
"v_exp_f32 v113, v113\n"
"v_add_f32 v105, v105, v107\n"
"v_pk_mul_f32 v[48:49], v[48:49], v[44:45] op_sel_hi:[1,0]\n"
"v_cvt_pk_bf16_f32 v20, v112, v113\n"
"v_pk_mul_f32 v[50:51], v[50:51], v[44:45] op_sel_hi:[1,0]\n"
"s_cbranch_vccnz LH_RETRY3%=\n"
"LH_C3%=:\n"
"v_pk_mul_f32 v[52:53], v[52:53], v[44:45] op_sel_hi:[1,0]\n"
"v_pk_mul_f32 v[54:55], v[54:55], v[44:45] op_sel_hi:[1,0]\n"
"v_pk_add_f32 v[108:109], v[108:109], v[110:111]\n"
"v_pk_add_f32 v[104:105], v[104:105], v[108:109]\n"
"v_mfma_f32_16x16x32_bf16 v[48:51], v[16:19], v[20:23], v[48:51]\n"
"v_add_f32 v112, v112, v113\n"
"v_add_f32 v104, v104, v105\n"
"v_mfma_f32_16x16x32_bf16 v[52:55], v[18:21], v[20:23], v[52:55]\n"
"v_fma_f32 v15, v104, v44, v112\n"
"v_mov_b32 v12, v15\n"
"s_nop 1\n"
"v_permlane16_swap_b32 v15, v12\n"
"v_add_f32 v15, v15, v12\n"
"v_mov_b32 v12, v15\n"
"s_add_u32 s50, s50, 1\n"
"v_mov_b64 v[74:75], 0\n"
"v_permlane32_swap_b32 v15, v12\n"
"v_add_f32 v24, v15, v12\n"
"v_rcp_f32 v24, v24\n"
"v_mov_b64 v[76:77], 0\n"
"v_pk_mul_f32 v[48:49], v[48:49], v[24:25] op_sel_hi:[1,0]\n"
"v_pk_mul_f32 v[50:51], v[50:51], v[24:25] op_sel_hi:[1,0]\n"
"v_cvt_pk_bf16_f32 v28, v48, v49\n"
"v_pk_mul_f32 v[52:53], v[52:53], v[24:25] op_sel_hi:[1,0]\n"
"v_cvt_pk_bf16_f32 v29, v50, v51\n"
"v_pk_mul_f32 v[54:55], v[54:55], v[24:25] op_sel_hi:[1,0]\n"
"v_cvt_pk_bf16_f32 v30, v52, v53\n"
"v_cvt_pk_bf16_f32 v31, v54, v55\n"
"ds_write_b128 v10, v[28:31]\n"
"v_mov_b64 v[78:79], 0\n"
"v_mov_b64 v[80:81], 0\n"
"v_mov_b64 v[82:83], 0\n"
"v_mov_b64 v[84:85], 0\n"
"v_mov_b64 v[86:87], 0\n"
"s_waitcnt lgkmcnt(0)\n"
"s_barrier\n"
"ds_read_b128 v[32:35], v0 offset:45312\n"
"ds_read_b128 v[36:39], v0 offset:46336\n"
"s_waitcnt lgkmcnt(1)\n"
"v_smfmac_f32_16x16x64_bf16 v[56:59], v[32:35], v[128:135], v14\n"
"v_smfmac_f32_16x16x64_bf16 v[60:63], v[32:35], v[136:143], v14\n"
"s_waitcnt lgkmcnt(0)\n"
"v_smfmac_f32_16x16x64_bf16 v[56:59], v[36:39], v[192:199], v14\n"
"v_smfmac_f32_16x16x64_bf16 v[60:63], v[36:39], v[200:207], v14\n"
"v_smfmac_f32_16x16x64_bf16 v[64:67], v[32:35], v[144:151], v14\n"
"v_smfmac_f32_16x16x64_bf16 v[64:67], v[36:39], v[208:215], v14\n"
"v_smfmac_f32_16x16x64_bf16 v[68:71], v[32:35], v[152:159], v14\n"
"v_smfmac_f32_16x16x64_bf16 v[68:71], v[36:39], v[216:223], v14\n"
"s_nop 0\n"
"s_nop 0\n"
"v_smfmac_f32_16x16x64_bf16 v[72:75], v[32:35], v[160:167], v14\n"
"v_add_f32 v56, v56, v58\n"
"v_smfmac_f32_16x16x64_bf16 v[72:75], v[36:39], v[224:231], v14\n"
"v_add_f32 v57, v57, v59\n"
"v_add_f32 v60, v60, v62\n"
"v_smfmac_f32_16x16x64_bf16 v[76:79], v[32:35], v[168:175], v14\n"
"v_add_f32 v61, v61, v63\n"
"v_cvt_pk_bf16_f32 v40, v56, v60\n"
"v_smfmac_f32_16x16x64_bf16 v[76:79], v[36:39], v[232:239], v14\n"
"v_add_f32 v64, v64, v66\n"
"v_cvt_pk_bf16_f32 v42, v57, v61\n"
"v_smfmac_f32_16x16x64_bf16 v[80:83], v[32:35], v[176:183], v14\n"
"v_add_f32 v65, v65, v67\n"
"v_add_f32 v68, v68, v70\n"
"v_smfmac_f32_16x16x64_bf16 v[80:83], v[36:39], v[240:247], v14\n"
"v_add_f32 v69, v69, v71\n"
"v_add_f32 v72, v72, v74\n"
"v_smfmac_f32_16x16x64_bf16 v[84:87], v[32:35], v[184:191], v14\n"
"v_add_f32 v73, v73, v75\n"
"v_cvt_pk_bf16_f32 v41, v64, v68\n"
"v_smfmac_f32_16x16x64_bf16 v[84:87], v[36:39], v[248:255], v14\n"
"v_cvt_pk_bf16_f32 v43, v65, v69\n"
"buffer_store_dwordx4 v[40:43], v11, s[40:43], s52 offen sc1\n"
"v_add_f32 v76, v76, v78\n"
"v_add_f32 v77, v77, v79\n"
"v_cvt_pk_bf16_f32 v44, v72, v76\n"
"v_cvt_pk_bf16_f32 v46, v73, v77\n"
"v_pk_add_f32 v[80:81], v[80:81], v[82:83]\n"
"s_nop 0\n"
"v_pk_add_f32 v[84:85], v[84:85], v[86:87]\n"
"v_cvt_pk_bf16_f32 v45, v80, v84\n"
"v_cvt_pk_bf16_f32 v47, v81, v85\n"
"buffer_store_dwordx4 v[44:47], v11, s[40:43], s53 offen sc1\n"
"s_memrealtime s[96:97]\n"
"s_waitcnt lgkmcnt(0)\n"
"s_mov_b32 s27, s96\n"
"s_xor_b32 s52, s52, 0x80000\n"
"s_xor_b32 s53, s53, 0x80000\n"
"s_sub_u32 s28, s27, s91\n"
"ds_read_b128 v[56:59], v1 offset:0\n"
"ds_read_b128 v[60:63], v1 offset:4096\n"
"ds_read_b128 v[64:67], v1 offset:1024\n"
"ds_read_b128 v[68:71], v1 offset:5120\n"
"ds_read_b128 v[72:75], v1 offset:2048\n"
"ds_read_b128 v[76:79], v1 offset:6144\n"
"ds_read_b128 v[80:83], v1 offset:3072\n"
"ds_read_b128 v[84:87], v1 offset:7168\n"
"ds_read_b128 v[120:123], v2 offset:8192\n"
"ds_read_b128 v[124:127], v2 offset:9216\n"
"v_xor_b32 v1, 0x4000, v1\n"
"v_xor_b32 v2, 0x4000, v2\n"
"v_mov_b64 v[104:105], 0\n"
"v_mov_b64 v[106:107], 0\n"
"v_mov_b64 v[108:109], 0\n"
"v_mov_b64 v[110:111], 0\n"
"v_mov_b64 v[112:113], 0\n"
"v_mov_b64 v[114:115], 0\n"
"v_mov_b64 v[116:117], 0\n"
"v_mov_b64 v[118:119], 0\n"
"s_barrier\n"
"s_lshr_b32 s73, s48, 2\n"
"s_or_b32 s73, s73, s49\n"
"s_cbranch_scc1 LH_NM%=\n"
"v_mov_b32 v35, s28\n"
"s_lshl_b32 s73, s48, 2\n"
"v_mov_b32 v34, s73\n"
"s_mov_b64 exec, 1\n"
"buffer_store_dword v35, v34, s[40:43], s93 offen offset:1024 sc1\n"
"s_mov_b64 exec, -1\n"
"LH_NM%=:\n"
"v_mov_b32 v34, 0\n"
"buffer_load_dwordx2 v[32:33], v34, s[40:43], s79 offen sc1\n"
"buffer_load_dwordx2 v[36:37], v34, s[40:43], s99 offen sc1\n"
"v_lshrrev_b32 v96, 1, v0\n"
"v_lshrrev_b32 v97, 9, v0\n"
"v_mul_u32_u24 v97, 288, v97\n"
"v_add_u32 v97, v97, v96\n"
"s_mul_i32 s73, s49, 1088\n"
"s_add_u32 s73, s73, 41216\n"
"v_add_u32 v97, s73, v97\n"
"ds_read_b64 v[98:99], v97\n"
"s_waitcnt lgkmcnt(0)\n"
"buffer_store_dwordx2 v[98:99], v96, s[40:43], s59 offen sc1\n"
"s_add_u32 s59, s59, 0x10000\n"
"s_cmp_lt_u32 s50, 63\n"
"s_cbranch_scc1 LH_DODMA%=\n"
"s_waitcnt vmcnt(0)\n"
"s_branch LH_NODMA%=\n"
"LH_DODMA%=:\n"
"s_add_u32 s26, s50, 1\n"
"s_bitcmp1_b64 s[80:81], s26\n"
"s_cbranch_scc1 LH_KVOK%=\n"
"v_mov_b32 v34, 0\n"
"s_mov_b32 s72, 0\n"
"LH_KVW%=:\n"
"s_waitcnt vmcnt(1)\n"
"buffer_load_dwordx2 v[36:37], v34, s[40:43], s99 offen sc1\n"
"s_waitcnt vmcnt(0) lgkmcnt(0)\n"
"v_readfirstlane_b32 s73, v36\n"
"v_readfirstlane_b32 s74, v37\n"
"s_add_u32 s72, s72, 1\n"
"s_cmp_gt_u32 s72, 400000\n"
"s_cbranch_scc1 LH_FAIL%=\n"
"s_mov_b32 s80, s73\n"
"s_mov_b32 s81, s74\n"
"s_bitcmp0_b64 s[80:81], s26\n"
"s_cbranch_scc1 LH_KVW%=\n"
"s_mov_b32 s72, 0\n"
"LH_KVOK%=:\n"
"s_mov_b32 m0, s66\n"
"s_add_u32 s73, s66, 8192\n"
"buffer_load_dwordx4 v0, s[40:43], s57 offen sc1 lds\n"
"buffer_load_dwordx4 v0, s[40:43], s57 offen offset:1024 sc1 lds\n"
"s_mov_b32 m0, s73\n"
"s_xor_b32 s66, s66, 0x4000\n"
"buffer_load_dwordx4 v0, s[40:43], s58 offen sc1 lds\n"
"buffer_load_dwordx4 v0, s[40:43], s58 offen offset:1024 sc1 lds\n"
"s_add_u32 s57, s57, 0x40000\n"
"s_add_u32 s58, s58, 0x40000\n"
"LH_NODMA%=:\n"
"s_waitcnt vmcnt(5)\n"
"v_readfirstlane_b32 s80, v36\n"
"v_readfirstlane_b32 s81, v37\n"
"v_readfirstlane_b32 s90, v32\n"
"v_readfirstlane_b32 s91, v33\n"
"s_mov_b32 m0, s87\n"
"s_memrealtime s[96:97]\n"
"s_waitcnt lgkmcnt(0)\n"
"s_sub_u32 s73, s91, s96\n"
"s_add_u32 s73, s73, 600\n"
"s_cmp_gt_u32 s73, 1600\n"
"s_cbranch_scc1 LH_FB2%=\n"
"LH_W2%=:\n"
"s_sub_u32 s73, s91, s96\n"
"s_cmp_le_i32 s73, 4\n"
"s_cbranch_scc1 LH_V2%=\n"
"s_memrealtime s[96:97]\n"
"s_waitcnt lgkmcnt(0)\n"
"s_branch LH_W2%=\n"
"LH_V2%=:\n"
"s_sub_u32 s73, s73, 0\n"
"s_cmp_le_i32 s73, 0\n"
"s_cbranch_scc1 LH_GO2%=\n"
"LH_U2%=:\n"
"s_sub_u32 s73, s73, 1\n"
"s_cmp_gt_i32 s73, 0\n"
"s_cbranch_scc1 LH_U2%=\n"
"s_branch LH_GO2%=\n"
"LH_FB2%=:\n"
"s_add_u32 s29, s29, 1\n"
"s_mov_b32 s72, s71\n"
"LH_D2%=:\n"
"s_sub_u32 s72, s72, 1\n"
"s_cmp_gt_i32 s72, 0\n"
"s_cbranch_scc1 LH_D2%=\n"
"LH_GO2%=:\n"
"buffer_load_dwordx4 v0, s[40:43], s56 offen sc1 lds\n"
"buffer_load_dwordx4 v0, s[40:43], s56 offen offset:1024 sc1 lds\n"
"s_mov_b32 s72, 0\n"
"s_mov_b32 s61, s56\n"
"s_mov_b32 s79, s92\n"
"s_and_b32 s73, s50, 7\n"
"s_lshl_b32 s74, s73, 7\n"
"s_add_u32 s92, s93, s74\n"
"s_lshl_b32 s73, s73, 14\n"
"s_add_u32 s55, s69, s73\n"
"s_add_u32 s56, s55, s87\n"
"s_and_b32 s73, s50, 3\n"
"s_lshl_b32 s73, s73, 17\n"
"s_add_u32 s67, s68, s73\n"
"s_waitcnt lgkmcnt(0)\n"
"s_waitcnt vmcnt(0)\n"
"s_branch LH_STEP%=\n"
"LH_RETRY2%=:\n"
"v_max3_u32 v15, v24, v25, v26\n"
"v_max3_u32 v15, v15, v27, v28\n"
"v_max3_u32 v15, v15, v29, v30\n"
"v_max3_u32 v15, v15, v31, v32\n"
"v_max3_u32 v15, v15, v33, v34\n"
"v_max3_u32 v15, v15, v35, v36\n"
"v_max3_u32 v15, v15, v37, v38\n"
"v_max3_u32 v15, v15, v39, v40\n"
"v_max3_u32 v15, v15, v41, v42\n"
"v_max3_u32 v15, v15, v43, v44\n"
"v_max3_u32 v15, v15, v45, v46\n"
"v_max3_u32 v15, v15, v47, v48\n"
"v_max3_u32 v15, v15, v49, v50\n"
"v_max3_u32 v15, v15, v51, v52\n"
"v_max3_u32 v15, v15, v53, v54\n"
"v_max_u32 v15, v15, v55\n"
"v_cmp_eq_u32 vcc, -1, v15\n"
"s_nop 1\n"
"s_cbranch_vccz LH_CONT2%=\n"
"s_or_b32 s95, s95, 2\n"
"s_add_u32 s77, s77, 1\n"
"s_add_u32 s72, s72, 1\n"
"s_cmp_gt_u32 s72, 400000\n"
"s_cbranch_scc1 LH_FAIL%=\n"
"s_waitcnt vmcnt(0) lgkmcnt(0)\n"
"s_barrier\n"
"s_mov_b32 m0, s87\n"
"s_nop 0\n"
"buffer_load_dwordx4 v0, s[40:43], s61 offen sc1 lds\n"
"buffer_load_dwordx4 v0, s[40:43], s61 offen offset:1024 sc1 lds\n"
"s_nop 7\n"
"v_mov_b64 v[104:105], 0\n"
"v_mov_b64 v[106:107], 0\n"
"v_mov_b64 v[108:109], 0\n"
"v_mov_b64 v[110:111], 0\n"
"v_mov_b64 v[112:113], 0\n"
"v_mov_b64 v[114:115], 0\n"
"v_mov_b64 v[116:117], 0\n"
"v_mov_b64 v[118:119], 0\n"
"s_waitcnt vmcnt(0) lgkmcnt(0)\n"
"s_nop 1\n"
"s_branch LH_STEP%=\n"
"LH_RETRY3%=:\n"
"s_add_u32 s86, s86, 1\n"
"s_add_u32 s72, s72, 1\n"
"s_cmp_gt_u32 s72, 400000\n"
"s_cbranch_scc1 LH_FAIL%=\n"
"buffer_load_dword v16, v13, s[40:43], s67 offen sc1\n"
"buffer_load_dword v18, v13, s[40:43], s67 offen offset:256 sc1\n"
"s_waitcnt vmcnt(0) lgkmcnt(0)\n"
"v_max_u32 v12, v16, v18\n"
"v_cmp_eq_u32 vcc, -1, v12\n"
"s_cbranch_vccnz LH_RETRY3%=\n"
"s_mov_b32 s72, 0\n"
"s_branch LH_C3%=\n"
"LH_DONE%=:\n"
"s_waitcnt vmcnt(0) lgkmcnt(0)\n"
"v_lshrrev_b32 v96, 1, v0\n"
"v_lshrrev_b32 v97, 9, v0\n"
"v_mul_u32_u24 v97, 288, v97\n"
"v_add_u32 v97, v97, v96\n"
"s_mul_i32 s73, s49, 1088\n"
"s_add_u32 s73, s73, 41216\n"
"v_add_u32 v97, s73, v97\n"
"ds_read_b64 v[98:99], v97\n"
"s_waitcnt lgkmcnt(0)\n"
"buffer_store_dwordx2 v[98:99], v96, s[40:43], s59 offen sc1\n"
"s_add_u32 s59, s59, 0x10000\n"
"s_waitcnt vmcnt(0) lgkmcnt(0)\n"
"s_branch LH_END%=\n"
"LH_FAIL%=:\n"
"s_mov_b32 s84, 0\n"
"v_mov_b32 v34, 0\n"
"v_mov_b32 v35, 1\n"
"s_mov_b32 s26, 256\n"
"s_mov_b64 exec, 1\n"
"buffer_store_dword v35, v34, s[40:43], s26 offen sc1\n"
"s_mov_b64 exec, -1\n"
"LH_END%=:\n"
"s_waitcnt vmcnt(0) lgkmcnt(0)\n"
   : "=&s"(lost),"+s"(tag) : "s"(kp),"s"(h),"s"(w),"s"(slot),"s"(blockIdx.x) : CHAIN_CLOB); } else { asm volatile(
"s_load_dwordx8 s[52:59], %2, 16\n"
"s_load_dwordx2 s[30:31], %2, 8\n"
"s_load_dwordx2 s[40:41], %2, 80\n"
"s_load_dwordx2 s[88:89], %2, 88\n"
"s_mov_b32 s42, 79691776\n"
"s_mov_b32 s43, 0x20000\n"
"s_mov_b32 s47, 0x20000\n"
"s_mov_b32 %0, 0\n"
"s_mov_b32 s48, %3\n"
"s_mov_b32 s49, %4\n"
"s_mov_b32 s70, 70\n"
"s_mov_b32 s71, 50\n"
"s_mov_b32 s51, 63\n"
"s_mov_b32 s84, 1\n"
"s_mov_b32 s78, 0\n"
"s_mov_b32 s50, 0\n"
"s_mov_b32 s76, 0\n"
"s_mov_b32 s77, 0\n"
"s_mov_b32 s86, 0\n"
"s_mov_b32 s29, 0\n"
"s_mov_b32 s72, 0\n"
"s_mov_b32 s95, 0\n"
"s_mov_b32 s62, 0x05040100\n"
"s_mov_b32 s63, 0x07060302\n"
"s_mov_b32 s64, 0x3e0293ee\n"
"s_mov_b32 s65, 0x3e0293ee\n"
"v_mbcnt_lo_u32_b32 v15, -1, 0\n"
"v_mbcnt_hi_u32_b32 v15, -1, v15\n"
"v_lshlrev_b32 v0, 4, v15\n"
"v_and_b32 v24, 15, v15\n"
"v_lshrrev_b32 v25, 4, v15\n"
"v_lshrrev_b32 v26, 1, v25\n"
"v_and_b32 v27, 1, v25\n"
"v_lshlrev_b32 v28, 18, v26\n"
"v_lshlrev_b32 v29, 16, v27\n"
"v_add_u32 v28, v28, v29\n"
"v_lshlrev_b32 v29, 4, v24\n"
"v_add_u32 v28, v28, v29\n"
"s_lshl_b32 s73, s48, 8\n"
"v_add_u32 v28, s73, v28\n"
"s_lshl_b32 s85, s49, 20\n"
"s_lshl_b32 s87, s49, 11\n"
"s_waitcnt lgkmcnt(0)\n"
"v_mov_b32 v22, 0\n"
"s_mov_b32 s26, 38400\n"
"buffer_load_dwordx2 v[20:21], v22, s[40:43], s26 offen sc1\n"
"s_getpc_b64 s[44:45]\n"
"LR_PC%=:\n"
"s_mov_b32 s46, LR_END%=-LR_PC%=\n"
"v_lshlrev_b32 v31, 6, v15\n"
"s_lshl_b32 s73, s49, 12\n"
"buffer_load_dword v23, v31, s[44:47], s73 offen\n"
"s_mov_b64 s[44:45], s[56:57]\n"
"s_mov_b32 s46, 4194304\n"
"s_add_u32 s73, s85, 0\n"
"buffer_load_dwordx4 v[128:131], v28, s[44:47], s73 offen\n"
"s_add_u32 s73, s85, 8192\n"
"buffer_load_dwordx4 v[132:135], v28, s[44:47], s73 offen\n"
"s_add_u32 s73, s85, 131072\n"
"buffer_load_dwordx4 v[136:139], v28, s[44:47], s73 offen\n"
"s_add_u32 s73, s85, 139264\n"
"buffer_load_dwordx4 v[140:143], v28, s[44:47], s73 offen\n"
"s_add_u32 s73, s85, 16384\n"
"buffer_load_dwordx4 v[144:147], v28, s[44:47], s73 offen\n"
"s_add_u32 s73, s85, 24576\n"
"buffer_load_dwordx4 v[148:151], v28, s[44:47], s73 offen\n"
"s_add_u32 s73, s85, 147456\n"
"buffer_load_dwordx4 v[152:155], v28, s[44:47], s73 offen\n"
"s_add_u32 s73, s85, 155648\n"
"buffer_load_dwordx4 v[156:159], v28, s[44:47], s73 offen\n"
"s_add_u32 s73, s85, 524288\n"
"buffer_load_dwordx4 v[160:163], v28, s[44:47], s73 offen\n"
"s_add_u32 s73, s85, 532480\n"
"buffer_load_dwordx4 v[164:167], v28, s[44:47], s73 offen\n"
"s_add_u32 s73, s85, 655360\n"
"buffer_load_dwordx4 v[168:171], v28, s[44:47], s73 offen\n"
"s_add_u32 s73, s85, 663552\n"
"buffer_load_dwordx4 v[172:175], v28, s[44:47], s73 offen\n"
"s_add_u32 s73, s85, 540672\n"
"buffer_load_dwordx4 v[176:179], v28, s[44:47], s73 offen\n"
"s_add_u32 s73, s85, 548864\n"
"buffer_load_dwordx4 v[180:183], v28, s[44:47], s73 offen\n"
"s_add_u32 s73, s85, 671744\n"
"buffer_load_dwordx4 v[184:187], v28, s[44:47], s73 offen\n"
"s_add_u32 s73, s85, 679936\n"
"buffer_load_dwordx4 v[188:191], v28, s[44:47], s73 offen\n"
"s_add_u32 s73, s85, 32768\n"
"buffer_load_dwordx4 v[192:195], v28, s[44:47], s73 offen\n"
"s_add_u32 s73, s85, 40960\n"
"buffer_load_dwordx4 v[196:199], v28, s[44:47], s73 offen\n"
"s_add_u32 s73, s85, 163840\n"
"buffer_load_dwordx4 v[200:203], v28, s[44:47], s73 offen\n"
"s_add_u32 s73, s85, 172032\n"
"buffer_load_dwordx4 v[204:207], v28, s[44:47], s73 offen\n"
"s_add_u32 s73, s85, 49152\n"
"buffer_load_dwordx4 v[208:211], v28, s[44:47], s73 offen\n"
"s_add_u32 s73, s85, 57344\n"
"buffer_load_dwordx4 v[212:215], v28, s[44:47], s73 offen\n"
"s_add_u32 s73, s85, 180224\n"
"buffer_load_dwordx4 v[216:219], v28, s[44:47], s73 offen\n"
"s_add_u32 s73, s85, 188416\n"
"buffer_load_dwordx4 v[220:223], v28, s[44:47], s73 offen\n"
"s_add_u32 s73, s85, 557056\n"
"buffer_load_dwordx4 v[224:227], v28, s[44:47], s73 offen\n"
"s_add_u32 s73, s85, 565248\n"
"buffer_load_dwordx4 v[228:231], v28, s[44:47], s73 offen\n"
"s_add_u32 s73, s85, 688128\n"
"buffer_load_dwordx4 v[232:235], v28, s[44:47], s73 offen\n"
"s_add_u32 s73, s85, 696320\n"
"buffer_load_dwordx4 v[236:239], v28, s[44:47], s73 offen\n"
"s_add_u32 s73, s85, 573440\n"
"buffer_load_dwordx4 v[240:243], v28, s[44:47], s73 offen\n"
"s_add_u32 s73, s85, 581632\n"
"buffer_load_dwordx4 v[244:247], v28, s[44:47], s73 offen\n"
"s_add_u32 s73, s85, 704512\n"
"buffer_load_dwordx4 v[248:251], v28, s[44:47], s73 offen\n"
"s_add_u32 s73, s85, 712704\n"
"buffer_load_dwordx4 v[252:255], v28, s[44:47], s73 offen\n"
"s_waitcnt vmcnt(33)\n"
"v_readfirstlane_b32 s66, v20\n"
"v_readfirstlane_b32 s67, v21\n"
"s_cmp_lt_i32 %5, 0\n"
"s_cbranch_scc1 LR_CT1%=\n"
"s_add_u32 s66, s66, 1\n"
"s_addc_u32 s67, s67, 0\n"
"s_bitset0_b32 s67, 31\n"
"s_mov_b64 %1, s[66:67]\n"
"LR_CT1%=:\n"
"s_mov_b64 s[66:67], %1\n"
"s_lshl_b64 s[68:69], s[66:67], 1\n"
"s_or_b32 s68, s68, 1\n"
"v_mov_b32 v20, s66\n"
"v_mov_b32 v21, s67\n"
"v_not_b32 v18, v20\n"
"v_mov_b32 v19, 0\n"
"s_lshl_b32 s73, %5, 3\n"
"s_add_u32 s73, s73, 38912\n"
"s_lshr_b32 s74, %5, 31\n"
"s_or_b32 s74, s74, s49\n"
"s_cselect_b32 s74, 0x80000000, 0\n"
"s_cselect_b32 s73, 0, s73\n"
"v_mov_b32 v17, s74\n"
"s_mov_b64 exec, 1\n"
"buffer_atomic_swap_x2 v[20:21], v17, s[40:43], s73 offen sc0 sc1\n"
"s_mov_b64 exec, -1\n"
"s_sub_u32 s73, s49, 0\n"
"s_lshl_b32 s73, s73, 1\n"
"s_and_b32 s73, s73, 7\n"
"s_lshl_b32 s73, s73, 12\n"
"s_add_u32 s73, s73, 0\n"
"v_add_u32 v32, s73, v0\n"
"s_sub_u32 s73, s49, 1\n"
"s_lshl_b32 s73, s73, 1\n"
"s_and_b32 s73, s73, 7\n"
"s_lshl_b32 s73, s73, 12\n"
"s_add_u32 s73, s73, 32768\n"
"v_add_u32 v33, s73, v0\n"
"s_sub_u32 s73, s49, 2\n"
"s_lshl_b32 s73, s73, 1\n"
"s_and_b32 s73, s73, 7\n"
"s_lshl_b32 s73, s73, 12\n"
"s_add_u32 s73, s73, 65536\n"
"v_add_u32 v34, s73, v0\n"
"s_sub_u32 s73, s49, 3\n"
"s_lshl_b32 s73, s73, 1\n"
"s_and_b32 s73, s73, 7\n"
"s_lshl_b32 s73, s73, 12\n"
"s_add_u32 s73, s73, 98304\n"
"v_add_u32 v35, s73, v0\n"
"s_lshl_b32 s73, s49, 15\n"
"v_add_u32 v2, s73, v0\n"
"s_waitcnt vmcnt(31)\n"
"v_perm_b32 v40, v132, v128, s62\n"
"v_perm_b32 v44, v132, v128, s63\n"
"v_perm_b32 v48, v133, v129, s62\n"
"v_perm_b32 v52, v133, v129, s63\n"
"v_perm_b32 v56, v134, v130, s62\n"
"v_perm_b32 v60, v134, v130, s63\n"
"v_perm_b32 v64, v135, v131, s62\n"
"v_perm_b32 v68, v135, v131, s63\n"
"s_waitcnt vmcnt(29)\n"
"v_perm_b32 v41, v140, v136, s62\n"
"v_perm_b32 v45, v140, v136, s63\n"
"v_perm_b32 v49, v141, v137, s62\n"
"v_perm_b32 v53, v141, v137, s63\n"
"v_perm_b32 v57, v142, v138, s62\n"
"v_perm_b32 v61, v142, v138, s63\n"
"v_perm_b32 v65, v143, v139, s62\n"
"v_perm_b32 v69, v143, v139, s63\n"
"s_waitcnt vmcnt(27)\n"
"v_perm_b32 v42, v148, v144, s62\n"
"v_perm_b32 v46, v148, v144, s63\n"
"v_perm_b32 v50, v149, v145, s62\n"
"v_perm_b32 v54, v149, v145, s63\n"
"v_perm_b32 v58, v150, v146, s62\n"
"v_perm_b32 v62, v150, v146, s63\n"
"v_perm_b32 v66, v151, v147, s62\n"
"v_perm_b32 v70, v151, v147, s63\n"
"s_waitcnt vmcnt(25)\n"
"v_perm_b32 v43, v156, v152, s62\n"
"v_perm_b32 v47, v156, v152, s63\n"
"v_perm_b32 v51, v157, v153, s62\n"
"v_perm_b32 v55, v157, v153, s63\n"
"v_perm_b32 v59, v158, v154, s62\n"
"v_perm_b32 v63, v158, v154, s63\n"
"v_perm_b32 v67, v159, v155, s62\n"
"v_perm_b32 v71, v159, v155, s63\n"
"ds_write_b128 v32, v[40:43] offset:0\n"
"ds_write_b128 v32, v[44:47] offset:2048\n"
"ds_write_b128 v33, v[48:51] offset:0\n"
"ds_write_b128 v33, v[52:55] offset:2048\n"
"ds_write_b128 v34, v[56:59] offset:0\n"
"ds_write_b128 v34, v[60:63] offset:2048\n"
"ds_write_b128 v35, v[64:67] offset:0\n"
"ds_write_b128 v35, v[68:71] offset:2048\n"
"s_waitcnt vmcnt(23)\n"
"v_perm_b32 v40, v164, v160, s62\n"
"v_perm_b32 v44, v164, v160, s63\n"
"v_perm_b32 v48, v165, v161, s62\n"
"v_perm_b32 v52, v165, v161, s63\n"
"v_perm_b32 v56, v166, v162, s62\n"
"v_perm_b32 v60, v166, v162, s63\n"
"v_perm_b32 v64, v167, v163, s62\n"
"v_perm_b32 v68, v167, v163, s63\n"
"s_waitcnt vmcnt(21)\n"
"v_perm_b32 v41, v172, v168, s62\n"
"v_perm_b32 v45, v172, v168, s63\n"
"v_perm_b32 v49, v173, v169, s62\n"
"v_perm_b32 v53, v173, v169, s63\n"
"v_perm_b32 v57, v174, v170, s62\n"
"v_perm_b32 v61, v174, v170, s63\n"
"v_perm_b32 v65, v175, v171, s62\n"
"v_perm_b32 v69, v175, v171, s63\n"
"s_waitcnt vmcnt(19)\n"
"v_perm_b32 v42, v180, v176, s62\n"
"v_perm_b32 v46, v180, v176, s63\n"
"v_perm_b32 v50, v181, v177, s62\n"
"v_perm_b32 v54, v181, v177, s63\n"
"v_perm_b32 v58, v182, v178, s62\n"
"v_perm_b32 v62, v182, v178, s63\n"
"v_perm_b32 v66, v183, v179, s62\n"
"v_perm_b32 v70, v183, v179, s63\n"
"s_waitcnt vmcnt(17)\n"
"v_perm_b32 v43, v188, v184, s62\n"
"v_perm_b32 v47, v188, v184, s63\n"
"v_perm_b32 v51, v189, v185, s62\n"
"v_perm_b32 v55, v189, v185, s63\n"
"v_perm_b32 v59, v190, v186, s62\n"
"v_perm_b32 v63, v190, v186, s63\n"
"v_perm_b32 v67, v191, v187, s62\n"
"v_perm_b32 v71, v191, v187, s63\n"
"ds_write_b128 v32, v[40:43] offset:1024\n"
"ds_write_b128 v32, v[44:47] offset:3072\n"
"ds_write_b128 v33, v[48:51] offset:1024\n"
"ds_write_b128 v33, v[52:55] offset:3072\n"
"ds_write_b128 v34, v[56:59] offset:1024\n"
"ds_write_b128 v34, v[60:63] offset:3072\n"
"ds_write_b128 v35, v[64:67] offset:1024\n"
"ds_write_b128 v35, v[68:71] offset:3072\n"
"s_waitcnt vmcnt(15)\n"
"v_perm_b32 v40, v196, v192, s62\n"
"v_perm_b32 v44, v196, v192, s63\n"
"v_perm_b32 v48, v197, v193, s62\n"
"v_perm_b32 v52, v197, v193, s63\n"
"v_perm_b32 v56, v198, v194, s62\n"
"v_perm_b32 v60, v198, v194, s63\n"
"v_perm_b32 v64, v199, v195, s62\n"
"v_perm_b32 v68, v199, v195, s63\n"
"s_waitcnt vmcnt(13)\n"
"v_perm_b32 v41, v204, v200, s62\n"
"v_perm_b32 v45, v204, v200, s63\n"
"v_perm_b32 v49, v205, v201, s62\n"
"v_perm_b32 v53, v205, v201, s63\n"
"v_perm_b32 v57, v206, v202, s62\n"
"v_perm_b32 v61, v206, v202, s63\n"
"v_perm_b32 v65, v207, v203, s62\n"
"v_perm_b32 v69, v207, v203, s63\n"
"s_waitcnt vmcnt(11)\n"
"v_perm_b32 v42, v212, v208, s62\n"
"v_perm_b32 v46, v212, v208, s63\n"
"v_perm_b32 v50, v213, v209, s62\n"
"v_perm_b32 v54, v213, v209, s63\n"
"v_perm_b32 v58, v214, v210, s62\n"
"v_perm_b32 v62, v214, v210, s63\n"
"v_perm_b32 v66, v215, v211, s62\n"
"v_perm_b32 v70, v215, v211, s63\n"
"s_waitcnt vmcnt(9)\n"
"v_perm_b32 v43, v220, v216, s62\n"
"v_perm_b32 v47, v220, v216, s63\n"
"v_perm_b32 v51, v221, v217, s62\n"
"v_perm_b32 v55, v221, v217, s63\n"
"v_perm_b32 v59, v222, v218, s62\n"
"v_perm_b32 v63, v222, v218, s63\n"
"v_perm_b32 v67, v223, v219, s62\n"
"v_perm_b32 v71, v223, v219, s63\n"
"ds_write_b128 v32, v[40:43] offset:4096\n"
"ds_write_b128 v32, v[44:47] offset:6144\n"
"ds_write_b128 v33, v[48:51] offset:4096\n"
"ds_write_b128 v33, v[52:55] offset:6144\n"
"ds_write_b128 v34, v[56:59] offset:4096\n"
"ds_write_b128 v34, v[60:63] offset:6144\n"
"ds_write_b128 v35, v[64:67] offset:4096\n"
"ds_write_b128 v35, v[68:71] offset:6144\n"
"s_waitcnt vmcnt(7)\n"
"v_perm_b32 v40, v228, v224, s62\n"
"v_perm_b32 v44, v228, v224, s63\n"
"v_perm_b32 v48, v229, v225, s62\n"
"v_perm_b32 v52, v229, v225, s63\n"
"v_perm_b32 v56, v230, v226, s62\n"
"v_perm_b32 v60, v230, v226, s63\n"
"v_perm_b32 v64, v231, v227, s62\n"
"v_perm_b32 v68, v231, v227, s63\n"
"s_waitcnt vmcnt(5)\n"
"v_perm_b32 v41, v236, v232, s62\n"
"v_perm_b32 v45, v236, v232, s63\n"
"v_perm_b32 v49, v237, v233, s62\n"
"v_perm_b32 v53, v237, v233, s63\n"
"v_perm_b32 v57, v238, v234, s62\n"
"v_perm_b32 v61, v238, v234, s63\n"
"v_perm_b32 v65, v239, v235, s62\n"
"v_perm_b32 v69, v239, v235, s63\n"
"s_waitcnt vmcnt(3)\n"
"v_perm_b32 v42, v244, v240, s62\n"
"v_perm_b32 v46, v244, v240, s63\n"
"v_perm_b32 v50, v245, v241, s62\n"
"v_perm_b32 v54, v245, v241, s63\n"
"v_perm_b32 v58, v246, v242, s62\n"
"v_perm_b32 v62, v246, v242, s63\n"
"v_perm_b32 v66, v247, v243, s62\n"
"v_perm_b32 v70, v247, v243, s63\n"
"s_waitcnt vmcnt(1)\n"
"v_perm_b32 v43, v252, v248, s62\n"
"v_perm_b32 v47, v252, v248, s63\n"
"v_perm_b32 v51, v253, v249, s62\n"
"v_perm_b32 v55, v253, v249, s63\n"
"v_perm_b32 v59, v254, v250, s62\n"
"v_perm_b32 v63, v254, v250, s63\n"
"v_perm_b32 v67, v255, v251, s62\n"
"v_perm_b32 v71, v255, v251, s63\n"
"ds_write_b128 v32, v[40:43] offset:5120\n"
"ds_write_b128 v32, v[44:47] offset:7168\n"
"ds_write_b128 v33, v[48:51] offset:5120\n"
"ds_write_b128 v33, v[52:55] offset:7168\n"
"ds_write_b128 v34, v[56:59] offset:5120\n"
"ds_write_b128 v34, v[60:63] offset:7168\n"
"ds_write_b128 v35, v[64:67] offset:5120\n"
"ds_write_b128 v35, v[68:71] offset:7168\n"
"s_waitcnt lgkmcnt(0)\n"
"s_barrier\n"
"ds_read_b128 a[0:3], v2 offset:0\n"
"ds_read_b128 a[4:7], v2 offset:1024\n"
"ds_read_b128 a[8:11], v2 offset:2048\n"
"ds_read_b128 a[12:15], v2 offset:3072\n"
"ds_read_b128 a[16:19], v2 offset:4096\n"
"ds_read_b128 a[20:23], v2 offset:5120\n"
"ds_read_b128 a[24:27], v2 offset:6144\n"
"ds_read_b128 a[28:31], v2 offset:7168\n"
"ds_read_b128 a[32:35], v2 offset:8192\n"
"ds_read_b128 a[36:39], v2 offset:9216\n"
"ds_read_b128 a[40:43], v2 offset:10240\n"
"ds_read_b128 a[44:47], v2 offset:11264\n"
"ds_read_b128 a[48:51], v2 offset:12288\n"
"ds_read_b128 a[52:55], v2 offset:13312\n"
"ds_read_b128 a[56:59], v2 offset:14336\n"
"ds_read_b128 a[60:63], v2 offset:15360\n"
"ds_read_b128 a[64:67], v2 offset:16384\n"
"ds_read_b128 a[68:71], v2 offset:17408\n"
"ds_read_b128 a[72:75], v2 offset:18432\n"
"ds_read_b128 a[76:79], v2 offset:19456\n"
"ds_read_b128 a[80:83], v2 offset:20480\n"
"ds_read_b128 a[84:87], v2 offset:21504\n"
"ds_read_b128 a[88:91], v2 offset:22528\n"
"ds_read_b128 a[92:95], v2 offset:23552\n"
"ds_read_b128 a[96:99], v2 offset:24576\n"
"ds_read_b128 a[100:103], v2 offset:25600\n"
"ds_read_b128 a[104:107], v2 offset:26624\n"
"ds_read_b128 a[108:111], v2 offset:27648\n"
"ds_read_b128 a[112:115], v2 offset:28672\n"
"ds_read_b128 a[116:119], v2 offset:29696\n"
"ds_read_b128 a[120:123], v2 offset:30720\n"
"ds_read_b128 a[124:127], v2 offset:31744\n"
"s_waitcnt lgkmcnt(0)\n"
"s_barrier\n"
"s_cmp_lt_i32 %5, 0\n"
"s_cbranch_scc1 LR_CC2%=\n"
"s_cmp_lg_u32 s49, 0\n"
"s_cbranch_scc1 LR_CC1%=\n"
"s_waitcnt vmcnt(0)\n"
"v_readfirstlane_b32 s28, v20\n"
"v_readfirstlane_b32 s29, v21\n"
"s_cmp_eq_u64 s[28:29], s[66:67]\n"
"s_cselect_b32 s28, 1, 0\n"
"s_mov_b32 s29, 0\n"
"s_and_b32 s73, s68, -2\n"
"s_or_b32 s73, s73, s28\n"
"v_mov_b32 v18, s73\n"
"v_mov_b32 v19, s69\n"
"v_mov_b32 v20, s28\n"
"s_lshl_b32 s73, %6, 3\n"
"s_add_u32 s73, s73, 40960\n"
"s_mov_b64 exec, 1\n"
"buffer_store_dwordx2 v[18:19], v22, s[40:43], s73 offen sc1\n"
"ds_write_b32 v22, v20\n"
"s_mov_b64 exec, -1\n"
"LR_CC1%=:\n"
"s_waitcnt lgkmcnt(0)\n"
"s_barrier\n"
"ds_read_b32 v20, v22\n"
"s_waitcnt lgkmcnt(0)\n"
"v_readfirstlane_b32 %0, v20\n"
"s_barrier\n"
"s_cmp_lg_u32 %0, 0\n"
"s_cbranch_scc1 LR_END%=\n"
"LR_CC2%=:\n"
"v_mov_b32 v34, 0\n"
"s_mov_b32 s72, 0\n"
"s_mov_b32 s26, 38656\n"
"LR_SEL%=:\n"
"buffer_load_dwordx2 v[36:37], v34, s[40:43], s26 offen sc1\n"
"s_waitcnt vmcnt(0) lgkmcnt(0)\n"
"v_readfirstlane_b32 s73, v36\n"
"v_readfirstlane_b32 s74, v37\n"
"s_add_u32 s72, s72, 1\n"
"s_cmp_gt_u32 s72, 400000\n"
"s_cbranch_scc1 LR_FAIL%=\n"
"s_mov_b32 s66, s73\n"
"s_mov_b32 s67, s74\n"
"s_or_b32 s66, s66, 1\n"
"s_cmp_lg_u64 s[66:67], s[68:69]\n"
"s_cbranch_scc1 LR_SEL%=\n"
"s_mov_b32 s72, 0\n"
"s_and_b32 s75, s73, 1\n"
"s_lshl_b32 s75, s75, 13\n"
"s_add_u32 s75, s75, 2097152\n"
"v_bfe_u32 v32, v24, 1, 1\n"
"v_bfe_u32 v34, v24, 3, 1\n"
"v_lshlrev_b32 v34, 1, v34\n"
"v_add_u32 v32, v32, v34\n"
"v_lshlrev_b32 v32, 4, v32\n"
"v_bfe_u32 v34, v24, 2, 1\n"
"v_lshlrev_b32 v34, 3, v34\n"
"v_add_u32 v32, v32, v34\n"
"v_and_b32 v34, 1, v24\n"
"v_lshlrev_b32 v34, 2, v34\n"
"v_add_u32 v32, v32, v34\n"
"s_lshl_b32 s73, s48, 8\n"
"s_lshl_b32 s74, s49, 6\n"
"s_add_u32 s73, s73, s74\n"
"v_add_u32 v32, s73, v32\n"
"v_mov_b32 v34, 0x80000000\n"
"v_cmp_eq_u32 vcc, 0, v25\n"
"s_nop 1\n"
"v_cndmask_b32 v11, v34, v32, vcc\n"
"v_bfe_u32 v32, v24, 1, 1\n"
"v_lshlrev_b32 v32, 1, v32\n"
"v_add_u32 v32, v32, v26\n"
"v_lshlrev_b32 v32, 4, v32\n"
"v_and_b32 v35, 1, v24\n"
"v_lshlrev_b32 v35, 3, v35\n"
"v_add_u32 v32, v32, v35\n"
"v_lshlrev_b32 v35, 2, v27\n"
"v_add_u32 v32, v32, v35\n"
"v_add_u32 v32, s73, v32\n"
"v_cmp_gt_u32 vcc, 4, v24\n"
"s_nop 1\n"
"v_cndmask_b32 v11, v34, v32, vcc\n"
"s_lshl_b32 s54, s48, 14\n"
"s_add_u32 s54, s54, s87\n"
"s_add_u32 s54, s54, s75\n"
"s_mov_b32 s83, s54\n"
"s_or_b32 s94, s48, s49\n"
"s_mov_b32 s80, 0\n"
"s_mov_b32 s81, 0\n"
"s_mov_b32 s57, 0\n"
"s_mov_b32 s85, 0\n"
"s_lshl_b32 s98, s89, 16\n"
"s_or_b32 s98, s98, s88\n"
"v_and_b32 v96, 16, v0\n"
"v_mov_b32 v97, 0x3f80\n"
"v_lshlrev_b32 v92, v96, v97\n"
"v_mov_b32 v93, v92\n"
"v_mov_b64 v[94:95], v[92:93]\n"
"v_lshrrev_b32 v96, 2, v24\n"
"v_mov_b32 v97, v92\n"
"v_cmp_eq_u32 vcc, 0, v96\n"
"s_nop 1\n"
"v_cndmask_b32 v92, 0, v97, vcc\n"
"v_cmp_eq_u32 vcc, 1, v96\n"
"s_nop 1\n"
"v_cndmask_b32 v93, 0, v97, vcc\n"
"v_cmp_eq_u32 vcc, 2, v96\n"
"s_nop 1\n"
"v_cndmask_b32 v94, 0, v97, vcc\n"
"v_cmp_eq_u32 vcc, 3, v96\n"
"s_nop 1\n"
"v_cndmask_b32 v95, 0, v97, vcc\n"
"v_mov_b64 v[88:89], -1\n"
"v_mov_b64 v[90:91], -1\n"
"v_lshrrev_b32 v10, 1, v0\n"
"v_mov_b32 v34, 0x80000000\n"
"v_cmp_eq_u32 vcc, 0, v15\n"
"s_nop 1\n"
"v_cndmask_b32 v9, v34, v15, vcc\n"
"v_mov_b32 v8, v9\n"
"s_cmp_eq_u32 s49, 0\n"
"s_cselect_b32 s73, 0, 0x80000000\n"
"v_or_b32 v9, s73, v9\n"
"s_cmp_eq_u32 s94, 0\n"
"s_cselect_b32 s73, 0, 0x80000000\n"
"v_or_b32 v8, s73, v8\n"
"v_add_u32 v3, s87, v0\n"
"s_lshl_b32 s73, s49, 1\n"
"s_add_u32 s73, s73, 2\n"
"s_and_b32 s73, s73, 7\n"
"s_lshl_b32 s73, s73, 10\n"
"v_add_u32 v4, s73, v0\n"
"s_lshl_b32 s73, s49, 1\n"
"s_add_u32 s73, s73, 4\n"
"s_and_b32 s73, s73, 7\n"
"s_lshl_b32 s73, s73, 10\n"
"v_add_u32 v5, s73, v0\n"
"s_lshl_b32 s73, s49, 1\n"
"s_add_u32 s73, s73, 6\n"
"s_and_b32 s73, s73, 7\n"
"s_lshl_b32 s73, s73, 10\n"
"v_add_u32 v6, s73, v0\n"
"v_lshlrev_b32 v13, 2, v15\n"
"v_bfe_u32 v32, v15, 1, 1\n"
"v_mul_u32_u24 v32, 0xaaaa, v32\n"
"v_add_u32 v14, 0x4444, v32\n"
"s_add_u32 s69, s75, 0x100000\n"
"s_mov_b32 s55, s69\n"
"s_add_u32 s56, s55, s87\n"
"s_mov_b32 s61, s56\n"
"s_lshr_b32 s73, s48, 2\n"
"s_lshl_b32 s73, s73, 14\n"
"s_and_b32 s74, s48, 3\n"
"s_lshl_b32 s74, s74, 11\n"
"s_add_u32 s73, s73, s74\n"
"s_lshl_b32 s74, s49, 9\n"
"s_add_u32 s73, s73, s74\n"
"s_add_u32 s73, s73, s75\n"
"s_add_u32 s68, s73, 0x140000\n"
"s_mov_b32 s67, s68\n"
"s_lshl_b32 s73, s48, 11\n"
"s_lshl_b32 s74, s49, 9\n"
"s_add_u32 s73, s73, s74\n"
"s_add_u32 s59, s73, 37748736\n"
"s_add_u32 s60, s73, 41943040\n"
"s_add_u32 s93, s75, 0x1c0000\n"
"s_mov_b32 s92, s93\n"
"s_add_u32 s79, s93, 896\n"
"v_lshrrev_b32 v32, 2, v24\n"
"v_lshlrev_b32 v32, 1, v32\n"
"v_and_b32 v34, 1, v24\n"
"v_add_u32 v32, v32, v34\n"
"v_lshlrev_b32 v32, 10, v32\n"
"v_lshlrev_b32 v34, 6, v25\n"
"v_add_u32 v32, v32, v34\n"
"v_bfe_u32 v34, v24, 1, 1\n"
"v_lshlrev_b32 v34, 5, v34\n"
"v_add_u32 v32, v32, v34\n"
"s_mov_b64 s[44:45], s[30:31]\n"
"s_mov_b32 s46, 8192\n"
"s_lshl_b32 s73, s49, 8\n"
"buffer_load_dwordx2 v[24:25], v32, s[44:47], s73 offen\n"
"buffer_load_dwordx2 v[26:27], v32, s[44:47], s73 offen offset:16\n"
"buffer_load_dwordx2 v[28:29], v32, s[44:47], s73 offen offset:8\n"
"buffer_load_dwordx2 v[30:31], v32, s[44:47], s73 offen offset:24\n"
"s_lshl_b32 s73, s48, 7\n"
"s_add_u32 s99, s73, 13312\n"
"s_add_u32 s73, s54, 0\n"
"buffer_store_dwordx4 v[88:91], v0, s[40:43], s73 offen sc1\n"
"buffer_store_dwordx4 v[88:91], v0, s[40:43], s73 offen offset:1024 sc1\n"
"s_add_u32 s73, s54, 524288\n"
"buffer_store_dwordx4 v[88:91], v0, s[40:43], s73 offen sc1\n"
"buffer_store_dwordx4 v[88:91], v0, s[40:43], s73 offen offset:1024 sc1\n"
"s_add_u32 s73, s69, 0\n"
"buffer_store_dword v88, v11, s[40:43], s73 offen sc1\n"
"s_add_u32 s73, s68, 0\n"
"buffer_store_dwordx2 v[88:89], v10, s[40:43], s73 offen sc1\n"
"s_add_u32 s73, s69, 16384\n"
"buffer_store_dword v88, v11, s[40:43], s73 offen sc1\n"
"s_add_u32 s73, s68, 131072\n"
"buffer_store_dwordx2 v[88:89], v10, s[40:43], s73 offen sc1\n"
"v_mov_b64 v[104:105], 0\n"
"v_mov_b64 v[106:107], 0\n"
"v_mov_b64 v[108:109], 0\n"
"v_mov_b64 v[110:111], 0\n"
"s_waitcnt vmcnt(10)\n"
"ds_write_b128 v3, v[24:27]\n"
"s_waitcnt vmcnt(8)\n"
"ds_write_b128 v3, v[28:31] offset:1024\n"
"s_waitcnt vmcnt(0) lgkmcnt(0)\n"
"s_barrier\n"
"v_mov_b32 v34, 0\n"
"v_mov_b32 v35, 1\n"
"s_mov_b32 s73, 0\n"
"s_cmp_lg_u32 s49, 0\n"
"s_cbranch_scc1 LR_S1%=\n"
"s_mov_b64 exec, 1\n"
"buffer_atomic_add v35, v34, s[40:43], s73 offen sc1\n"
"s_mov_b64 exec, -1\n"
"LR_S1%=:\n"
"s_cmp_lg_u32 s94, 0\n"
"s_cbranch_scc1 LR_S3%=\n"
"v_mov_b32 v34, 0\n"
"s_mov_b32 s72, 0\n"
"s_mov_b32 s26, 0\n"
"LR_S2%=:\n"
"buffer_load_dword v36, v34, s[40:43], s26 offen sc1\n"
"s_waitcnt vmcnt(0) lgkmcnt(0)\n"
"v_readfirstlane_b32 s73, v36\n"
"s_add_u32 s72, s72, 1\n"
"s_cmp_gt_u32 s72, 400000\n"
"s_cbranch_scc1 LR_FAIL%=\n"
"s_cmp_lt_u32 s73, 64\n"
"s_cbranch_scc1 LR_S2%=\n"
"s_mov_b32 s72, 0\n"
"v_and_b32 v35, 31, v15\n"
"v_lshlrev_b32 v35, 7, v35\n"
"s_mov_b32 s26, 1024\n"
"LR_S6%=:\n"
"buffer_load_dword v36, v35, s[40:43], s26 offen sc1\n"
"s_waitcnt vmcnt(0) lgkmcnt(0)\n"
"v_and_b32 v36, 3, v36\n"
"v_cmp_ne_u32 vcc, 3, v36\n"
"s_add_u32 s72, s72, 1\n"
"s_cmp_gt_u32 s72, 400000\n"
"s_cbranch_scc1 LR_FAIL%=\n"
"s_cbranch_vccnz LR_S6%=\n"
"s_mov_b32 s72, 0\n"
"s_memrealtime s[96:97]\n"
"s_waitcnt vmcnt(0) lgkmcnt(0)\n"
"s_add_u32 s28, s96, 70\n"
"s_add_u32 s73, s28, s88\n"
"s_add_u32 s74, s73, s89\n"
"v_mov_b32 v98, s73\n"
"v_mov_b32 v99, s74\n"
"v_mov_b32 v100, 0\n"
"s_mov_b64 exec, 1\n"
"buffer_store_dwordx2 v[98:99], v100, s[40:43], s92 offen offset:896 sc1\n"
"s_mov_b64 exec, -1\n"
"v_or_b32 v99, 1, v99\n"
"s_mov_b32 s26, 512\n"
"s_mov_b64 exec, 1\n"
"buffer_store_dwordx2 v[98:99], v100, s[40:43], s26 offen sc1\n"
"s_mov_b64 exec, -1\n"
"LR_S3%=:\n"
"v_mov_b32 v34, 0\n"
"s_mov_b32 s72, 0\n"
"s_mov_b32 s26, 512\n"
"LR_S4%=:\n"
"buffer_load_dwordx2 v[36:37], v34, s[40:43], s26 offen sc1\n"
"s_waitcnt vmcnt(0) lgkmcnt(0)\n"
"v_readfirstlane_b32 s73, v36\n"
"v_readfirstlane_b32 s74, v37\n"
"s_add_u32 s72, s72, 1\n"
"s_cmp_gt_u32 s72, 400000\n"
"s_cbranch_scc1 LR_FAIL%=\n"
"s_cmp_eq_u32 s74, 0\n"
"s_cbranch_scc1 LR_S4%=\n"
"s_mov_b32 s72, 0\n"
"s_mov_b32 s90, s73\n"
"s_sub_u32 s91, s90, s88\n"
"s_sub_u32 s91, s91, 30\n"
"LR_S5%=:\n"
"s_memrealtime s[96:97]\n"
"s_waitcnt vmcnt(0) lgkmcnt(0)\n"
"s_sub_u32 s73, s91, s96\n"
"s_add_u32 s73, s73, 19\n"
"s_cmp_lt_u32 s73, 1000\n"
"s_cbranch_scc1 LR_S5%=\n"
"s_mov_b32 s78, s96\n"
"s_mov_b32 s72, 0\n"
"LR_STEP%=:\n"
"s_barrier\n"
"ds_read_b128 v[24:27], v3 offset:0\n"
"ds_read_b128 v[28:31], v3 offset:1024\n"
"ds_read_b128 v[32:35], v4 offset:0\n"
"s_waitcnt lgkmcnt(2)\n"
"v_smfmac_f32_16x16x64_bf16 v[104:107], v[24:27], a[0:7], v14\n"
"ds_read_b128 v[36:39], v4 offset:1024\n"
"v_smfmac_f32_16x16x64_bf16 v[108:111], v[24:27], a[8:15], v14\n"
"ds_read_b128 v[40:43], v5 offset:0\n"
"s_waitcnt lgkmcnt(3)\n"
"v_smfmac_f32_16x16x64_bf16 v[104:107], v[28:31], a[16:23], v14\n"
"ds_read_b128 v[44:47], v5 offset:1024\n"
"v_smfmac_f32_16x16x64_bf16 v[108:111], v[28:31], a[24:31], v14\n"
"ds_read_b128 v[48:51], v6 offset:0\n"
"s_waitcnt lgkmcnt(4)\n"
"v_smfmac_f32_16x16x64_bf16 v[104:107], v[32:35], a[32:39], v14\n"
"ds_read_b128 v[52:55], v6 offset:1024\n"
"v_smfmac_f32_16x16x64_bf16 v[108:111], v[32:35], a[40:47], v14\n"
"s_waitcnt lgkmcnt(4)\n"
"v_smfmac_f32_16x16x64_bf16 v[104:107], v[36:39], a[48:55], v14\n"
"v_smfmac_f32_16x16x64_bf16 v[108:111], v[36:39], a[56:63], v14\n"
"s_waitcnt lgkmcnt(0)\n"
"v_smfmac_f32_16x16x64_bf16 v[104:107], v[40:43], a[64:71], v14\n"
"v_smfmac_f32_16x16x64_bf16 v[104:107], v[44:47], a[80:87], v14\n"
"v_smfmac_f32_16x16x64_bf16 v[104:107], v[48:51], a[96:103], v14\n"
"v_smfmac_f32_16x16x64_bf16 v[104:107], v[52:55], a[112:119], v14\n"
"v_smfmac_f32_16x16x64_bf16 v[108:111], v[40:43], a[72:79], v14\n"
"v_smfmac_f32_16x16x64_bf16 v[108:111], v[44:47], a[88:95], v14\n"
"s_nop 1\n"
"v_smfmac_f32_16x16x64_bf16 v[108:111], v[48:51], a[104:111], v14\n"
"s_nop 1\n"
"v_smfmac_f32_16x16x64_bf16 v[108:111], v[52:55], a[120:127], v14\n"
"v_add_f32 v104, v104, v106\n"
"v_add_f32 v105, v105, v107\n"
"v_cmp_u_f32 vcc, v104, v105\n"
"v_cvt_pk_bf16_f32 v16, v104, v105\n"
"s_nop 1\n"
"s_cbranch_vccnz LR_RETRY2%=\n"
"LR_CONT2%=:\n"
"buffer_store_dword v16, v13, s[40:43], s67 offen sc1\n"
"v_pk_add_f32 v[108:109], v[108:109], v[110:111]\n"
"v_cvt_pk_bf16_f32 v17, v108, v109\n"
"buffer_store_dword v17, v13, s[40:43], s67 offen offset:256 sc1\n"
"buffer_store_dwordx2 v[16:17], v10, s[40:43], s60 offen sc1\n"
"s_add_u32 s60, s60, 0x10000\n"
"s_add_u32 s85, s85, 1\n"
"s_cmp_lt_u32 s85, 6\n"
"s_cbranch_scc1 LR_NDC%=\n"
"s_mov_b32 s85, 0\n"
"s_sub_u32 s80, s80, 3\n"
"s_max_i32 s80, s80, 0\n"
"s_lshr_b32 s73, s98, 16\n"
"s_sub_u32 s89, s89, 3\n"
"s_max_u32 s89, s89, s73\n"
"LR_NDC%=:\n"
"s_cmp_lt_u32 s95, 2\n"
"s_cselect_b32 s73, 1, s94\n"
"s_cmp_lg_u32 s73, 0\n"
"s_cbranch_scc1 LR_NRP%=\n"
"s_mov_b32 s85, 0\n"
"s_bitcmp1_b32 s95, 0\n"
"s_cselect_b32 s73, 3, 0\n"
"s_add_u32 s88, s88, s73\n"
"s_add_u32 s80, s80, s73\n"
"s_xor_b32 s73, s73, 3\n"
"s_add_u32 s89, s89, s73\n"
"s_memrealtime s[96:97]\n"
"s_waitcnt lgkmcnt(0)\n"
"s_add_u32 s28, s96, 4294967286\n"
"s_mov_b32 s26, s92\n"
"s_mov_b32 s92, s79\n"
"s_add_u32 s73, s28, s88\n"
"s_add_u32 s74, s73, s89\n"
"v_mov_b32 v98, s73\n"
"v_mov_b32 v99, s74\n"
"v_mov_b32 v100, 0\n"
"s_mov_b64 exec, 1\n"
"buffer_store_dwordx2 v[98:99], v100, s[40:43], s92 offen sc1\n"
"s_mov_b64 exec, -1\n"
"s_mov_b32 s92, s26\n"
"LR_NRP%=:\n"
"s_cmp_eq_u32 s50, s51\n"
"s_cbranch_scc1 LR_DONE%=\n"
"s_barrier\n"
"s_cmp_lt_u32 s95, 2\n"
"s_cbranch_scc1 LR_NOSL%=\n"
"s_sleep 10\n"
"LR_NOSL%=:\n"
"s_mov_b32 s95, 0\n"
"s_sleep 8\n"
"v_mov_b32 v34, 0\n"
"buffer_load_dwordx2 v[32:33], v34, s[40:43], s79 offen sc1\n"
"s_mov_b32 s26, 384\n"
"buffer_load_dword v56, v34, s[40:43], s26 offen sc1\n"
"buffer_load_dwordx4 v[36:39], v34, s[40:43], s93 offen offset:1024 sc1\n"
"buffer_store_dwordx4 v[88:91], v0, s[40:43], s83 offen sc1\n"
"buffer_store_dwordx4 v[88:91], v0, s[40:43], s83 offen offset:1024 sc1\n"
"s_add_u32 s73, s50, 2\n"
"s_and_b32 s74, s73, 7\n"
"s_lshl_b32 s74, s74, 14\n"
"s_add_u32 s74, s69, s74\n"
"buffer_store_dword v88, v11, s[40:43], s74 offen sc1\n"
"s_and_b32 s73, s73, 3\n"
"s_lshl_b32 s73, s73, 17\n"
"s_add_u32 s73, s68, s73\n"
"buffer_store_dwordx2 v[88:89], v10, s[40:43], s73 offen sc1\n"
"v_mov_b64 v[104:105], 0\n"
"v_mov_b64 v[106:107], 0\n"
"v_mov_b64 v[108:109], 0\n"
"v_mov_b64 v[110:111], 0\n"
"s_xor_b32 s54, s54, 0x80000\n"
"s_xor_b32 s83, s83, 0x80000\n"
"s_add_u32 s50, s50, 1\n"
"s_waitcnt vmcnt(6)\n"
"v_readfirstlane_b32 s90, v32\n"
"v_readfirstlane_b32 s91, v33\n"
"s_waitcnt vmcnt(4)\n"
"v_max3_u32 v36, v36, v37, v38\n"
"v_max_u32 v36, v36, v39\n"
"s_nop 0\n"
"v_readfirstlane_b32 s73, v36\n"
"s_and_b32 s74, s98, 0xffff\n"
"s_add_u32 s26, s74, 14\n"
"s_sub_u32 s74, s74, 20\n"
"s_add_u32 s73, s73, 6\n"
"s_cmp_gt_u32 s73, s26\n"
"s_cbranch_scc1 LR_NMU%=\n"
"s_max_u32 s73, s73, s74\n"
"s_max_u32 s88, s73, s81\n"
"s_add_u32 s88, s88, s80\n"
"s_mov_b32 s81, s73\n"
"LR_NMU%=:\n"
"s_cmp_eq_u32 s50, 1\n"
"s_cselect_b32 s82, 44, 0\n"
"v_readfirstlane_b32 s73, v56\n"
"s_cmp_ge_u32 s73, 20\n"
"s_addc_u32 s57, s57, 0\n"
"s_sub_u32 s73, s57, 1\n"
"s_cmp_lt_u32 s73, 5\n"
"s_cselect_b32 s82, 10, s82\n"
"s_memrealtime s[96:97]\n"
"s_waitcnt lgkmcnt(0)\n"
"s_sub_u32 s73, s90, s96\n"
"s_add_u32 s73, s73, 600\n"
"s_cmp_gt_u32 s73, 1600\n"
"s_cbranch_scc1 LR_FB1%=\n"
"s_add_u32 s73, s91, s88\n"
"s_add_u32 s73, s73, s82\n"
"s_add_u32 s74, s73, s89\n"
"s_lshr_b32 s26, s82, 2\n"
"s_add_u32 s74, s74, s26\n"
"v_mov_b32 v98, s73\n"
"v_mov_b32 v99, s74\n"
"buffer_store_dwordx2 v[98:99], v8, s[40:43], s92 offen sc1\n"
"LR_W1%=:\n"
"s_sub_u32 s73, s90, s96\n"
"s_cmp_le_i32 s73, 4\n"
"s_cbranch_scc1 LR_V1%=\n"
"s_memrealtime s[96:97]\n"
"s_waitcnt lgkmcnt(0)\n"
"s_branch LR_W1%=\n"
"LR_V1%=:\n"
"s_sub_u32 s73, s73, 0\n"
"s_cmp_le_i32 s73, 0\n"
"s_cbranch_scc1 LR_GO1%=\n"
"LR_U1%=:\n"
"s_sub_u32 s73, s73, 1\n"
"s_cmp_gt_i32 s73, 0\n"
"s_cbranch_scc1 LR_U1%=\n"
"s_branch LR_GO1%=\n"
"LR_FB1%=:\n"
"s_add_u32 s29, s29, 1\n"
"s_add_u32 s91, s96, 200\n"
"s_add_u32 s73, s91, s88\n"
"s_add_u32 s73, s73, s82\n"
"s_add_u32 s74, s73, s89\n"
"s_lshr_b32 s26, s82, 2\n"
"s_add_u32 s74, s74, s26\n"
"v_mov_b32 v98, s73\n"
"v_mov_b32 v99, s74\n"
"buffer_store_dwordx2 v[98:99], v8, s[40:43], s92 offen sc1\n"
"s_mov_b32 s72, s70\n"
"LR_D1%=:\n"
"s_sub_u32 s72, s72, 1\n"
"s_cmp_gt_i32 s72, 0\n"
"s_cbranch_scc1 LR_D1%=\n"
"LR_GO1%=:\n"
"LR_P1%=:\n"
"buffer_load_dwordx4 v[40:43], v0, s[40:43], s54 offen sc1\n"
"buffer_load_dwordx4 v[44:47], v0, s[40:43], s54 offen offset:1024 sc1\n"
"v_mov_b32 v34, 0\n"
"s_waitcnt vmcnt(0)\n"
"v_mfma_f32_16x16x32_bf16 v[48:51], v[92:95], v[40:43], 0\n"
"v_max3_u32 v15, v40, v41, v42\n"
"v_mfma_f32_16x16x32_bf16 v[48:51], v[92:95], v[44:47], v[48:51]\n"
"v_max3_u32 v12, v43, v44, v45\n"
"v_max3_u32 v15, v15, v46, v47\n"
"v_max_u32 v15, v15, v12\n"
"v_cmp_eq_u32 vcc, -1, v15\n"
"s_nop 3\n"
"v_add_f32_dpp v48, v48, v48 row_ror:8 row_mask:0xf bank_mask:0xf\n"
"v_add_f32_dpp v49, v49, v49 row_ror:8 row_mask:0xf bank_mask:0xf\n"
"s_cbranch_vccnz LR_RETRY1%=\n"
"v_add_f32_dpp v48, v48, v48 row_ror:4 row_mask:0xf bank_mask:0xf\n"
"v_add_f32_dpp v49, v49, v49 row_ror:4 row_mask:0xf bank_mask:0xf\n"
"v_cvt_pk_bf16_f32 v52, v48, v49\n"
"buffer_store_dword v52, v11, s[40:43], s55 offen sc1\n"
"s_mov_b32 m0, s87\n"
"s_memrealtime s[96:97]\n"
"s_waitcnt lgkmcnt(0)\n"
"s_sub_u32 s73, s91, s96\n"
"s_add_u32 s73, s73, 600\n"
"s_cmp_gt_u32 s73, 1600\n"
"s_cbranch_scc1 LR_FB2%=\n"
"LR_W2%=:\n"
"s_sub_u32 s73, s91, s96\n"
"s_cmp_le_i32 s73, 4\n"
"s_cbranch_scc1 LR_V2%=\n"
"s_memrealtime s[96:97]\n"
"s_waitcnt lgkmcnt(0)\n"
"s_branch LR_W2%=\n"
"LR_V2%=:\n"
"s_sub_u32 s73, s73, 0\n"
"s_cmp_le_i32 s73, 0\n"
"s_cbranch_scc1 LR_GO2%=\n"
"LR_U2%=:\n"
"s_sub_u32 s73, s73, 1\n"
"s_cmp_gt_i32 s73, 0\n"
"s_cbranch_scc1 LR_U2%=\n"
"s_branch LR_GO2%=\n"
"LR_FB2%=:\n"
"s_add_u32 s29, s29, 1\n"
"s_mov_b32 s72, 24\n"
"LR_D2%=:\n"
"s_sub_u32 s72, s72, 1\n"
"s_cmp_gt_i32 s72, 0\n"
"s_cbranch_scc1 LR_D2%=\n"
"LR_GO2%=:\n"
"buffer_load_dwordx4 v0, s[40:43], s56 offen sc1 lds\n"
"buffer_load_dwordx4 v0, s[40:43], s56 offen offset:1024 sc1 lds\n"
"s_mov_b32 s72, 0\n"
"s_mov_b32 s61, s56\n"
"s_mov_b32 s79, s92\n"
"s_and_b32 s73, s50, 7\n"
"s_lshl_b32 s74, s73, 7\n"
"s_add_u32 s92, s93, s74\n"
"s_lshl_b32 s73, s73, 14\n"
"s_add_u32 s55, s69, s73\n"
"s_add_u32 s56, s55, s87\n"
"s_and_b32 s73, s50, 3\n"
"s_lshl_b32 s73, s73, 17\n"
"s_add_u32 s67, s68, s73\n"
"s_waitcnt lgkmcnt(0)\n"
"s_waitcnt vmcnt(0)\n"
"s_branch LR_STEP%=\n"
"LR_RETRY1%=:\n"
"s_or_b32 s95, s95, 1\n"
"s_add_u32 s76, s76, 1\n"
"s_add_u32 s72, s72, 1\n"
"s_cmp_gt_u32 s72, 400000\n"
"s_cbranch_scc1 LR_FAIL%=\n"
"s_branch LR_P1%=\n"
"LR_RETRY2%=:\n"
"v_max3_u32 v15, v24, v25, v26\n"
"v_max3_u32 v15, v15, v27, v28\n"
"v_max3_u32 v15, v15, v29, v30\n"
"v_max3_u32 v15, v15, v31, v32\n"
"v_max3_u32 v15, v15, v33, v34\n"
"v_max3_u32 v15, v15, v35, v36\n"
"v_max3_u32 v15, v15, v37, v38\n"
"v_max3_u32 v15, v15, v39, v40\n"
"v_max3_u32 v15, v15, v41, v42\n"
"v_max3_u32 v15, v15, v43, v44\n"
"v_max3_u32 v15, v15, v45, v46\n"
"v_max3_u32 v15, v15, v47, v48\n"
"v_max3_u32 v15, v15, v49, v50\n"
"v_max3_u32 v15, v15, v51, v52\n"
"v_max3_u32 v15, v15, v53, v54\n"
"v_max_u32 v15, v15, v55\n"
"v_cmp_eq_u32 vcc, -1, v15\n"
"s_nop 1\n"
"s_cbranch_vccz LR_CONT2%=\n"
"s_or_b32 s95, s95, 2\n"
"s_add_u32 s77, s77, 1\n"
"s_add_u32 s72, s72, 1\n"
"s_cmp_gt_u32 s72, 400000\n"
"s_cbranch_scc1 LR_FAIL%=\n"
"s_waitcnt vmcnt(0) lgkmcnt(0)\n"
"s_barrier\n"
"s_mov_b32 m0, s87\n"
"s_nop 0\n"
"buffer_load_dwordx4 v0, s[40:43], s61 offen sc1 lds\n"
"buffer_load_dwordx4 v0, s[40:43], s61 offen offset:1024 sc1 lds\n"
"s_nop 7\n"
"v_mov_b64 v[104:105], 0\n"
"v_mov_b64 v[106:107], 0\n"
"v_mov_b64 v[108:109], 0\n"
"v_mov_b64 v[110:111], 0\n"
"s_waitcnt vmcnt(0) lgkmcnt(0)\n"
"s_nop 1\n"
"s_branch LR_STEP%=\n"
"LR_DONE%=:\n"
"s_waitcnt vmcnt(0) lgkmcnt(0)\n"
"s_waitcnt vmcnt(0) lgkmcnt(0)\n"
"s_branch LR_END%=\n"
"LR_FAIL%=:\n"
"s_mov_b32 s84, 0\n"
"v_mov_b32 v34, 0\n"
"v_mov_b32 v35, 1\n"
"s_mov_b32 s26, 256\n"
"s_mov_b64 exec, 1\n"
"buffer_store_dword v35, v34, s[40:43], s26 offen sc1\n"
"s_mov_b64 exec, -1\n"
"LR_END%=:\n"
"s_waitcnt vmcnt(0) lgkmcnt(0)\n"
   : "=&s"(lost),"+s"(tag) : "s"(kp),"s"(h),"s"(w),"s"(slot),"s"(blockIdx.x) : CHAIN_CLOB); } }

  } else {
    asm volatile("" : "+s"(kp));
    const Params p = get_params(kp);
    char* ws = p.ws;
    if (PASS == 0) {
      tag = mk_tag(__hip_atomic_load(ctl64(ws, C_E), RLX));
      if (blockIdx.x == 255 && w == 1) init_ctl(ws, tag, lane_id());
      lost = share(lds, w == 0 && claim_lost(ws, claim_try(ws, slot, tag, lane_id()), tag, lane_id()), w);
    }
    if (!lost) {
      if (x < 4) {
        if (PASS == 0 && (p.pad & 4)) CODE_WARM(CL("b"));
        if (y == 16 && x == 0 && w == 0) probe_sel(ws, tag);
        for (int i = 0; i < p.db; ++i) __builtin_amdgcn_s_sleep(16);
        if (p.pad & 1) {
          const int l = lane_id(), hh = ((y - 16) >> 1) * 4 + x, part = (y & 1) * 4 + w;
          warm(p.Wq, 4096 * 1024, (l >> 4) * 8192 + (l & 15) * 16, hh * 256 + part * 64 * 8192, 4 * 8192, 16, false);
          warm(p.Wo, 4096 * 1024, l * 16, hh * 131072 + part * 16384, 1024, 16, true);
        }
        if (wait_z(ws, tag)) role_b2(p, ws, (y - 16) * 4 + x, w);
        if (PASS == 0) CODE_END(CL("b"));
      } else {
        if (PASS == 0 && (p.pad & 4)) CODE_WARM(CL("d"));
        if (p.pad & 2) {
          const int l = lane_id();
          warm(p.Wv, 4096 * 1024, (l >> 4) * 8192 + (l & 15) * 16, ((x - 4) * 8 + y - 24) * 256 + w * 128 * 8192, 4 * 8192, 32, true);
        }
        for (int i = 0; i < p.dd; ++i) __builtin_amdgcn_s_sleep(16);
        if (wait_z(ws, tag)) {
          role_b1(p, ws, lds, (y - 24) * 4 + x - 4, w);
          role_d(p, ws, lds, (x - 4) >> 1, x & 1, y - 24, w);
        }
        if (PASS == 0) CODE_END(CL("d"));
      }
    }
  }
  return lost;
}
template <int V>
__global__ void __launch_bounds__(256, 1) mega(Params) {
  extern __shared__ __attribute__((aligned(16))) char smem[];
  LAS char* lds = (LAS char*)smem;
  kptr kp = (kptr)__builtin_amdgcn_kernarg_segment_ptr();
  const int w = __builtin_amdgcn_readfirstlane(threadIdx.x >> 6);
  int b = (blockIdx.x & ~7) | (__builtin_amdgcn_s_getreg(20 | (3 << 11)) & 7);
  u64 tag = 0;
  if (run_role<V, 0>(kp, lds, b, w, tag)) {
    asm volatile("" : "+s"(kp));
    b = adopt_role(kp->ws, tag, lane_id());
    if (b >= 0) run_role<V, 1>(kp, lds, b, w, tag);
  }
  asm volatile("" : "+s"(kp));
  if (blockIdx.x == 255 && w == 1) close_ctl(kp->ws, tag, lane_id());
}
template <int V>
static void launch(void* const* d_in, void* d_out, void* d_ws, hipStream_t stream, int P1, int P2, int da, int db, int dd) {
  char* ws = (char*)(((uintptr_t)d_ws + (2u << 20) - 1) & ~(uintptr_t)((2u << 20) - 1));
  Params p;
  p.x = (const u16*)d_in[0]; p.mem0 = (const u16*)d_in[1]; p.Wq = (const u16*)d_in[2]; p.Wk = (const u16*)d_in[3];
  p.Wv = (const u16*)d_in[4]; p.Wo = (const u16*)d_in[5]; p.wqkv = (const u16*)d_in[8]; p.wo = (const u16*)d_in[9];
  p.gw = (const u16*)d_in[10]; p.out = (u16*)d_out; p.ws = ws; p.P1 = P1; p.P2 = P2; p.da = da; p.db = db; p.dd = dd & 255; p.pad = dd >> 8;
  (void)hipFuncSetAttribute((const void*)mega<V>, hipFuncAttributeMaxDynamicSharedMemorySize, LDS_BYTES);
  mega<V><<<256, 256, LDS_BYTES, stream>>>(p);
}

extern "C" void kernel_launch(void* const* d_in, const int* in_sizes, int n_in, void* d_out, int out_size, void* d_ws, size_t ws_size,
                              hipStream_t stream) {
  (void)in_sizes; (void)n_in; (void)out_size; (void)ws_size;
  launch<0>(d_in, d_out, d_ws, stream, 128, 46, 4, 5, 2 + 256 * 7);
}
```
